```python
import torch

def _softmax(x, axis=-1):
    return torch.softmax(x, dim=axis)

def _gelu(x):
    return torch.nn.functional.gelu(x, approximate='tanh')

def _silu(x):
    return torch.nn.functional.silu(x)

def _sigmoid(x):
    return torch.sigmoid(x)

def _layernorm(x, axis=-1, eps=1e-05):
    m = x.mean(dim=axis, keepdim=True)
    v = x.var(dim=axis, keepdim=True, unbiased=False)
    return (x - m) / torch.sqrt(v + eps)

def _scatter_add(tgt, idx, src, axis=0):
    return tgt.clone().index_add_(axis, idx, src)

def _segment_sum(src, seg, n):
    out = torch.zeros((n,) + src.shape[1:], dtype=src.dtype)
    return out.index_add_(0, seg, src)

def _pad(x, pw):
    flat = tuple((v for pair in reversed(pw) for v in pair))
    return torch.nn.functional.pad(x, flat)
kernel_name = 'standalone__tree_speculative_verify_tree_attention_over_candidate_token_tree__sampling'

def setup_inputs():

    _w = lambda a: (lambda t: t.to(torch.bfloat16) if t.is_floating_point() else t)(torch.as_tensor(a))
    B, Hq, Hkv, T, C, D = (8, 16, 8, 32, 2048, 256)
    tree_mask = torch.zeros((B, T, T), dtype=float)
    for b in range(B):
        parent = torch.zeros(T, dtype=torch.int64)
        for i in range(1, T):
            parent[i] = int(torch.randint(0, i, (1,)).item())
        for i in range(T):
            j = i
            tree_mask[b, i, j] = 1.0
            while j > 0:
                j = parent[j]
                tree_mask[b, i, j] = 1.0
    return {'q': _w((lambda *s: torch.randn(*s, dtype=torch.bfloat16) * 0.8)(B, Hq, T, D)), 'k_cache': _w((lambda *s: torch.randn(*s, dtype=torch.bfloat16) * 0.8)(B, Hkv, C, D)), 'v_cache': _w((lambda *s: torch.randn(*s, dtype=torch.bfloat16) * 0.05)(B, Hkv, C, D)), 'k_tree': _w((lambda *s: torch.randn(*s, dtype=torch.bfloat16) * 0.8)(B, Hkv, T, D)), 'v_tree': _w((lambda *s: torch.randn(*s, dtype=torch.bfloat16) * 0.05)(B, Hkv, T, D)), 'tree_mask': _w(tree_mask)}

def reference(q, k_cache, v_cache, k_tree, v_tree, tree_mask):
    B, Hq, T, D = q.shape
    Hkv, C = (k_cache.shape[1], k_cache.shape[2])
    rep = Hq // Hkv
    kc = (lambda x, n, axis: torch.repeat_interleave(x, n, dim=axis))(k_cache, rep, axis=1)
    vc = (lambda x, n, axis: torch.repeat_interleave(x, n, dim=axis))(v_cache, rep, axis=1)
    kt = (lambda x, n, axis: torch.repeat_interleave(x, n, dim=axis))(k_tree, rep, axis=1)
    vt = (lambda x, n, axis: torch.repeat_interleave(x, n, dim=axis))(v_tree, rep, axis=1)
    k = torch.concatenate([kc, kt], axis=2)
    v = torch.concatenate([vc, vt], axis=2)
    scale = D ** (-0.5)
    scores = torch.einsum('bhqd,bhkd->bhqk', q, k) * scale
    ctx_mask = torch.ones((B, 1, T, C), dtype=scores.dtype)
    tm = tree_mask[:, None, :, :].clone()
    full_mask = torch.concatenate([ctx_mask, tm], axis=-1)
    scores = scores + (1.0 - full_mask) * -1000000000.0
    p = _softmax(scores, axis=-1)
    return torch.einsum('bhqk,bhkd->bhqd', p, v)
```

```cpp
#include <hip/hip_runtime.h>
#include <hip/hip_bf16.h>

typedef __attribute__((ext_vector_type(8))) short s16x8;
typedef __attribute__((ext_vector_type(4))) short s16x4;
typedef __attribute__((ext_vector_type(4))) float f32x4;
typedef __attribute__((ext_vector_type(2))) float f32x2;
typedef __attribute__((ext_vector_type(2))) __bf16 bf16x2;
typedef __attribute__((ext_vector_type(4))) unsigned u32x4;
typedef __attribute__((ext_vector_type(2))) unsigned u32x2;
typedef unsigned long long u64;
#define LAS __attribute__((address_space(3)))
#define GAS __attribute__((address_space(1)))

namespace {
constexpr int NB = 8, HQ = 16, HKV = 8, NT = 32, NC = 2048, ND = 256;
constexpr int KEYS = 512;
constexpr int SLOT = 16384;
#define RING 8
#define INFL 2
constexpr int NST = 32;
constexpr int LDS_TK = RING * SLOT;
constexpr int LDS_TV = LDS_TK + 4096;
constexpr int LDS_CTL = LDS_TV + 4096;
constexpr int LDS_BYTES = LDS_CTL + 64;
constexpr size_t WS_MSG = 0;
constexpr size_t WS_FLAG = (size_t)64 * 4 * 4 * 8192;
constexpr size_t WS_EPOCH = WS_FLAG + (size_t)64 * 4 * 4 * 4 * 128;
constexpr float LOG2E = 1.4426950408889634f;
constexpr float SC = 0.0625f * LOG2E;
constexpr unsigned SPIN_CAP = 1u << 22;

__device__ __forceinline__ unsigned pk_bf16(float a, float b) {
  f32x2 x = {a, b};
  return __builtin_bit_cast(unsigned, __builtin_convertvector(x, bf16x2));
}
__device__ __forceinline__ float bf_lo(unsigned u) { return __builtin_bit_cast(float, u << 16); }
__device__ __forceinline__ float bf_hi(unsigned u) { return __builtin_bit_cast(float, u & 0xffff0000u); }
__device__ __forceinline__ float xmax(float x) {
  auto s = __builtin_amdgcn_permlane16_swap(__float_as_uint(x), __float_as_uint(x), false, false);
  x = fmaxf(__uint_as_float(s[0]), __uint_as_float(s[1]));
  auto t = __builtin_amdgcn_permlane32_swap(__float_as_uint(x), __float_as_uint(x), false, false);
  return fmaxf(__uint_as_float(t[0]), __uint_as_float(t[1]));
}
__device__ __forceinline__ void dma16(unsigned voff, const void* sbase, unsigned ldsaddr) {
  asm volatile("s_mov_b32 m0, %2\n\ts_nop 0\n\tglobal_load_lds_dwordx4 %0, %1 nt" ::"v"(voff), "s"(sbase), "s"(ldsaddr) : "memory", "m0");
}
template <int N> __device__ __forceinline__ void wait_vm() { asm volatile("s_waitcnt vmcnt(%0)" ::"i"(N) : "memory"); }
__device__ __forceinline__ void wait_lgkm0() { asm volatile("s_waitcnt lgkmcnt(0)" ::: "memory"); }
}

__global__ __launch_bounds__(320, 2) void tree_attn_kernel(const unsigned short* __restrict__ q, const unsigned short* __restrict__ kc,
                                                           const unsigned short* __restrict__ vc, const unsigned short* __restrict__ kt,
                                                           const unsigned short* __restrict__ vt, const unsigned short* __restrict__ mask,
                                                           unsigned short* __restrict__ out, char* __restrict__ ws) {
  extern __shared__ __attribute__((aligned(16))) char lds_raw[];
  LAS char* lds = (LAS char*)lds_raw;
  const unsigned lds0 = (unsigned)(uintptr_t)lds;
  volatile LAS unsigned* ctl = (volatile LAS unsigned*)(lds + LDS_CTL);
  const int tid = threadIdx.x, lane = tid & 63;
  const int wave = __builtin_amdgcn_readfirstlane(tid >> 6);
  const int bid = blockIdx.x;
  const int kvh = bid & 7, split = (bid >> 3) & 3, b = bid >> 5;
  const int head = b * HKV + kvh;

  if (wave == 4) {
    if (lane < 8) ctl[lane] = 0u;
    const char* kb = (const char*)(kc + ((size_t)head * NC + (size_t)split * KEYS) * ND);
    const char* vb = (const char*)(vc + ((size_t)head * NC + (size_t)split * KEYS) * ND);
    unsigned koff[8];
#pragma unroll
    for (int p = 0; p < 8; ++p) {
      const int row = 2 * p + (lane >> 5);
      koff[p] = row * 512 + (((lane & 31) ^ (row & 15)) << 4);
    }
    const unsigned voff = (lane >> 3) * 512 + (((lane & 7) ^ (((lane >> 4) & 3) << 1)) << 4);
    auto issue = [&](int st) {
      const unsigned dst = lds0 + (st % RING) * SLOT;
      if (st < 16) {
        const char* src = kb + (size_t)st * SLOT;
#pragma unroll
        for (int p = 0; p < 16; ++p) dma16(koff[p & 7], src + (p >> 3) * 8192, dst + p * 1024);
      } else {
        const int jj = (st - 16) >> 2, i = st & 3;
        const int blk = (split + 1 + jj) & 3;
        const char* src = vb + (size_t)i * 65536 + blk * 128;
#pragma unroll
        for (int p = 0; p < 16; ++p) dma16(voff, src + p * 4096, dst + p * 1024);
      }
    };
    issue(0);
    {
      const char* tk = (const char*)(kt + ((size_t)head * NT + split * 8) * ND);
      const char* tv = (const char*)(vt + ((size_t)head * NT + split * 8) * ND);
#pragma unroll
      for (int p = 0; p < 4; ++p) dma16(koff[p], tk, lds0 + LDS_TK + p * 1024);
#pragma unroll
      for (int p = 0; p < 4; ++p) dma16((unsigned)(lane * 16 + p * 1024), tv, lds0 + LDS_TV + p * 1024);
    }
    wait_lgkm0();
    __builtin_amdgcn_s_barrier();
    int pub = 0;
#pragma unroll 1
    for (int st = 1; st < NST; ++st) {
      if (st >= RING) {
        const unsigned need = (unsigned)(st - RING + 1);
        unsigned spins = 0;
        for (;;) {
          const u32x4 dn = *(volatile LAS u32x4*)(lds + LDS_CTL + 16);
          const unsigned mn = min(min(dn[0], dn[1]), min(dn[2], dn[3]));
          if ((unsigned)__builtin_amdgcn_readfirstlane(mn) >= need) break;
          if (pub < st) {
            wait_vm<0>();
            pub = st;
            ctl[0] = (unsigned)pub;
          }
          __builtin_amdgcn_s_sleep(1);
          if (++spins > SPIN_CAP) break;
        }
      }
      issue(st);
      if (st + 1 - pub > INFL) {
        if (st == INFL) wait_vm<16 * INFL + 8>(); else wait_vm<16 * INFL>();
        ++pub;
        ctl[0] = (unsigned)pub;
      }
    }
    if (INFL > 2 && NST - pub > 2) { wait_vm<32>(); pub = NST - 2; ctl[0] = (unsigned)pub; }
    if (INFL > 1 && NST - pub > 1) { wait_vm<16>(); pub = NST - 1; ctl[0] = (unsigned)pub; }
    wait_vm<0>();
    ctl[0] = (unsigned)NST;
    return;
  }

  const int r = lane & 15, g = lane >> 4;
  const size_t qrow = ((size_t)b * HQ + kvh * 2) * NT + wave * 16 + r;
  s16x8 qf[8];
#pragma unroll
  for (int ks = 0; ks < 8; ++ks) qf[ks] = *(const s16x8*)(q + qrow * ND + ks * 32 + g * 8);
  GAS unsigned* epoch_p = (GAS unsigned*)(ws + WS_EPOCH + (size_t)(head * 4 + wave) * 128);
  const unsigned tag = __hip_atomic_load(epoch_p, __ATOMIC_RELAXED, __HIP_MEMORY_SCOPE_AGENT) + 1u;
  const int trow = (wave & 1) * 16 + r;
  const u32x2 mk = *(const u32x2*)(mask + ((size_t)b * NT + trow) * NT + split * 8 + (g & 1) * 4);
  __builtin_amdgcn_s_barrier();

  unsigned seen = 0;
  auto wait_full = [&](int st) {
    if (seen <= (unsigned)st) {
      unsigned spins = 0;
      for (;;) {
        seen = (unsigned)__builtin_amdgcn_readfirstlane(ctl[0]);
        if (seen > (unsigned)st) break;
        __builtin_amdgcn_s_sleep(1);
        if (++spins > SPIN_CAP) break;
      }
    }
    asm volatile("" ::: "memory");
  };
  auto release = [&](int st) {
    wait_lgkm0();
    ctl[4 + wave] = (unsigned)(st + 1);
  };

  f32x4 S[32];
  unsigned kaddr[4];
#pragma unroll
  for (int k4 = 0; k4 < 4; ++k4) kaddr[k4] = r * 512 + (((4 * k4 + g) ^ r) << 4);
#pragma unroll
  for (int st = 0; st < 16; ++st) {
    wait_full(st);
    s16x8 kf[16];
#pragma unroll
    for (int t = 0; t < 2; ++t)
#pragma unroll
      for (int ks = 0; ks < 8; ++ks)
        kf[t * 8 + ks] = *(const LAS s16x8*)(lds + (st % RING) * SLOT + t * 8192 + (ks >> 2) * 256 + kaddr[ks & 3]);
    release(st);
#pragma unroll
    for (int t = 0; t < 2; ++t) {
      f32x4 a = {0.f, 0.f, 0.f, 0.f};
#pragma unroll
      for (int ks = 0; ks < 8; ++ks) a = __builtin_amdgcn_mfma_f32_16x16x32_bf16(kf[t * 8 + ks], qf[ks], a, 0, 0, 0);
      S[2 * st + t] = a;
    }
  }
  f32x4 St = {0.f, 0.f, 0.f, 0.f};
  {
    const int r7 = r & 7;
#pragma unroll
    for (int ks = 0; ks < 8; ++ks) {
      const s16x8 kf = *(const LAS s16x8*)(lds + LDS_TK + r7 * 512 + (((4 * ks + g) ^ r7) << 4));
      St = __builtin_amdgcn_mfma_f32_16x16x32_bf16(kf, qf[ks], St, 0, 0, 0);
    }
  }
  float tt[4];
  {
    const float mv[4] = {bf_lo(mk[0]), bf_hi(mk[0]), bf_lo(mk[1]), bf_hi(mk[1])};
#pragma unroll
    for (int i = 0; i < 4; ++i) {
      const float bias = (1.0f - mv[i]) * -1000000000.0f;
      tt[i] = g < 2 ? (St[i] * 0.0625f + bias) * LOG2E : -__builtin_inff();
    }
  }
  float mx = S[0][0];
#pragma unroll
  for (int t = 0; t < 32; ++t)
#pragma unroll
    for (int i = 0; i < 4; ++i) mx = fmaxf(mx, S[t][i]);
  mx *= SC;
  mx = fmaxf(fmaxf(mx, fmaxf(tt[0], tt[1])), fmaxf(tt[2], tt[3]));
  mx = xmax(mx);
  u32x4 P[16];
#pragma unroll
  for (int u = 0; u < 16; ++u) {
    float e[8];
#pragma unroll
    for (int i = 0; i < 8; ++i) e[i] = __builtin_amdgcn_exp2f(fmaf(S[2 * u + (i >> 2)][i & 3], SC, -mx));
    P[u] = u32x4{pk_bf16(e[0], e[1]), pk_bf16(e[2], e[3]), pk_bf16(e[4], e[5]), pk_bf16(e[6], e[7])};
  }
  u32x2 Pt = {pk_bf16(__builtin_amdgcn_exp2f(tt[0] - mx), __builtin_amdgcn_exp2f(tt[1] - mx)),
              pk_bf16(__builtin_amdgcn_exp2f(tt[2] - mx), __builtin_amdgcn_exp2f(tt[3] - mx))};
  float lsum;
  {
    const s16x8 ones8 = {0x3f80, 0x3f80, 0x3f80, 0x3f80, 0x3f80, 0x3f80, 0x3f80, 0x3f80};
    const s16x4 ones4 = {0x3f80, 0x3f80, 0x3f80, 0x3f80};
    f32x4 la = {0.f, 0.f, 0.f, 0.f};
    la = __builtin_amdgcn_mfma_f32_16x16x16bf16_1k(ones4, __builtin_bit_cast(s16x4, Pt), la, 0, 0, 0);
#pragma unroll
    for (int u = 0; u < 16; ++u) la = __builtin_amdgcn_mfma_f32_16x16x32_bf16(ones8, __builtin_bit_cast(s16x8, P[u]), la, 0, 0, 0);
    lsum = la[0];
  }
  const float rl = 1.0f / lsum;
  const float lse = mx + __builtin_amdgcn_logf(lsum);

  const int qq = (lane & 15) >> 2, pp = lane & 3;
  unsigned vaddr[4];
  {
    const int x = (2 * g + (qq >> 1)) & 3;
#pragma unroll
    for (int t = 0; t < 4; ++t) vaddr[t] = (4 * g + qq) * 128 + ((t ^ x) << 5) + pp * 8;
  }
  const unsigned tvaddr = LDS_TV + ((4 * g + qq) & 7) * 512 + pp * 8;
  const auto msg_rsrc = __builtin_amdgcn_make_buffer_rsrc((void*)(ws + WS_MSG + (size_t)head * (16 * 8192)), 0, 16 * 8192, 0x00020000);
  GAS u64* flag_base = (GAS u64*)(ws + WS_FLAG + (size_t)head * (16 * 4 * 128) + wave * 128 + r * 8);
  f32x4 O[4];
  u32x4 X[3][2];
  float xl[3];
#pragma unroll 1
  for (int jj = 0; jj < 4; ++jj) {
    const int blk = (split + 1 + jj) & 3;
#pragma unroll
    for (int t = 0; t < 4; ++t) {
      const s16x4 v4 = __builtin_amdgcn_ds_read_tr16_b64_v4i16((LAS s16x4*)(lds + tvaddr + blk * 128 + t * 32));
      O[t] = __builtin_amdgcn_mfma_f32_16x16x16bf16_1k(v4, __builtin_bit_cast(s16x4, Pt), f32x4{0.f, 0.f, 0.f, 0.f}, 0, 0, 0);
    }
#pragma unroll
    for (int i = 0; i < 4; ++i) {
      const int st = 16 + jj * 4 + i;
      wait_full(st);
      const unsigned sbase = (unsigned)((st % RING) * SLOT);
      s16x4 vf[4][4][2];
#pragma unroll
      for (int u = 0; u < 4; ++u)
#pragma unroll
        for (int t = 0; t < 4; ++t)
#pragma unroll
          for (int h = 0; h < 2; ++h)
            vf[u][t][h] = __builtin_amdgcn_ds_read_tr16_b64_v4i16((LAS s16x4*)(lds + sbase + vaddr[t] + u * 4096 + h * 2048));
      release(st);
#pragma unroll
      for (int u = 0; u < 4; ++u)
#pragma unroll
        for (int t = 0; t < 4; ++t)
          O[t] = __builtin_amdgcn_mfma_f32_16x16x32_bf16(__builtin_shufflevector(vf[u][t][0], vf[u][t][1], 0, 1, 2, 3, 4, 5, 6, 7),
                                                         __builtin_bit_cast(s16x8, P[4 * i + u]), O[t], 0, 0, 0);
      if (i == 0 && jj > 0) {
        wait_vm<0>();
        const int pblk = (split + jj) & 3;
        if (g == 0) __hip_atomic_store(flag_base + (pblk * 4 + split) * 64, ((u64)tag << 32) | __float_as_uint(lse), __ATOMIC_RELAXED, __HIP_MEMORY_SCOPE_AGENT);
      }
      if (i == 1 && jj == 3) {
        unsigned spins = 0;
        for (;;) {
          bool ok = true;
#pragma unroll
          for (int p = 0; p < 3; ++p) {
            const int prod = (split + 1 + p) & 3;
            const u64 f = __hip_atomic_load(flag_base + (split * 4 + prod) * 64, __ATOMIC_RELAXED, __HIP_MEMORY_SCOPE_AGENT);
            xl[p] = __uint_as_float((unsigned)f);
            ok &= (unsigned)(f >> 32) == tag;
          }
          if (__all(ok)) break;
          __builtin_amdgcn_s_sleep(2);
          if (++spins > (SPIN_CAP >> 4)) break;
        }
        __builtin_amdgcn_fence(__ATOMIC_ACQUIRE, "agent");
#pragma unroll
        for (int p = 0; p < 3; ++p) {
          const int prod = (split + 1 + p) & 3;
          const int off = (split * 4 + prod) * 8192 + wave * 2048 + lane * 16;
          X[p][0] = __builtin_amdgcn_raw_buffer_load_b128(msg_rsrc, off, 0, 16);
          X[p][1] = __builtin_amdgcn_raw_buffer_load_b128(msg_rsrc, off + 1024, 0, 16);
        }
      }
    }
    if (jj < 3) {
      u32x4 w0 = {pk_bf16(O[0][0] * rl, O[0][1] * rl), pk_bf16(O[0][2] * rl, O[0][3] * rl), pk_bf16(O[1][0] * rl, O[1][1] * rl), pk_bf16(O[1][2] * rl, O[1][3] * rl)};
      u32x4 w1 = {pk_bf16(O[2][0] * rl, O[2][1] * rl), pk_bf16(O[2][2] * rl, O[2][3] * rl), pk_bf16(O[3][0] * rl, O[3][1] * rl), pk_bf16(O[3][2] * rl, O[3][3] * rl)};
      const int off = (blk * 4 + split) * 8192 + wave * 2048 + lane * 16;
      __builtin_amdgcn_raw_buffer_store_b128(w0, msg_rsrc, off, 0, 16);
      __builtin_amdgcn_raw_buffer_store_b128(w1, msg_rsrc, off + 1024, 0, 16);
    }
  }
  const float M = fmaxf(fmaxf(lse, xl[0]), fmaxf(xl[1], xl[2]));
  float w0 = __builtin_amdgcn_exp2f(lse - M), w1 = __builtin_amdgcn_exp2f(xl[0] - M), w2 = __builtin_amdgcn_exp2f(xl[1] - M), w3 = __builtin_amdgcn_exp2f(xl[2] - M);
  const float inv = 1.0f / (w0 + w1 + w2 + w3);
  w0 *= inv * rl; w1 *= inv; w2 *= inv; w3 *= inv;
  unsigned short* orow = out + qrow * ND + split * 64 + g * 4;
#pragma unroll
  for (int t = 0; t < 4; ++t) {
    const unsigned a0 = X[0][t >> 1][(t & 1) * 2], a1 = X[0][t >> 1][(t & 1) * 2 + 1];
    const unsigned b0 = X[1][t >> 1][(t & 1) * 2], b1 = X[1][t >> 1][(t & 1) * 2 + 1];
    const unsigned c0 = X[2][t >> 1][(t & 1) * 2], c1 = X[2][t >> 1][(t & 1) * 2 + 1];
    const float o0 = O[t][0] * w0 + bf_lo(a0) * w1 + bf_lo(b0) * w2 + bf_lo(c0) * w3;
    const float o1 = O[t][1] * w0 + bf_hi(a0) * w1 + bf_hi(b0) * w2 + bf_hi(c0) * w3;
    const float o2 = O[t][2] * w0 + bf_lo(a1) * w1 + bf_lo(b1) * w2 + bf_lo(c1) * w3;
    const float o3 = O[t][3] * w0 + bf_hi(a1) * w1 + bf_hi(b1) * w2 + bf_hi(c1) * w3;
    *(u32x2*)(orow + t * 16) = u32x2{pk_bf16(o0, o1), pk_bf16(o2, o3)};
  }
  if (split == 0 && lane == 0) __hip_atomic_store(epoch_p, tag, __ATOMIC_RELAXED, __HIP_MEMORY_SCOPE_AGENT);
}

extern "C" void kernel_launch(void* const* d_in, const int* in_sizes, int n_in, void* d_out, int out_size, void* d_ws, size_t ws_size,
                              hipStream_t stream) {
  (void)in_sizes; (void)n_in; (void)out_size; (void)ws_size;
  (void)hipFuncSetAttribute((const void*)tree_attn_kernel, hipFuncAttributeMaxDynamicSharedMemorySize, LDS_BYTES);
  tree_attn_kernel<<<256, 320, LDS_BYTES, stream>>>((const unsigned short*)d_in[0], (const unsigned short*)d_in[1], (const unsigned short*)d_in[2],
                                                    (const unsigned short*)d_in[3], (const unsigned short*)d_in[4], (const unsigned short*)d_in[5],
                                                    (unsigned short*)d_out, (char*)d_ws);
}
```

```cpp
#include <hip/hip_runtime.h>
#include <hip/hip_bf16.h>

typedef __attribute__((ext_vector_type(8))) short s16x8;
typedef __attribute__((ext_vector_type(4))) short s16x4;
typedef __attribute__((ext_vector_type(4))) float f32x4;
typedef __attribute__((ext_vector_type(2))) float f32x2;
typedef __attribute__((ext_vector_type(2))) __bf16 bf16x2;
typedef __attribute__((ext_vector_type(4))) unsigned u32x4;
typedef __attribute__((ext_vector_type(2))) unsigned u32x2;
typedef unsigned long long u64;
#define LAS __attribute__((address_space(3)))
#define GAS __attribute__((address_space(1)))

namespace {
constexpr int HQ = 16, HKV = 8, NT = 32, NC = 2048, ND = 256;
constexpr int KEYS = 512;
constexpr int SLOT = 16384;
#define RING 7
#define INFL 2
constexpr int NST = 32;
constexpr int MSG = 8192 + 256;
constexpr int LDS_TK = RING * SLOT;
constexpr int LDS_TV = LDS_TK + 4096;
constexpr int LDS_OUT = LDS_TV + 4096;
constexpr int LDS_IN = LDS_OUT + MSG;
constexpr int LDS_CTL = LDS_IN + 3 * MSG;
constexpr int LDS_BYTES = LDS_CTL + 64;
enum { C_FULL = 0, C_OUTFREE = 1, C_INREADY = 2, C_DONE = 4, C_OUTCNT = 8 };
constexpr int MSG_STRIDE = 8704;
constexpr size_t WS_MSG = 0;
constexpr size_t WS_FLAG = (size_t)64 * 16 * MSG_STRIDE;
constexpr size_t WS_EPOCH = WS_FLAG + (size_t)64 * 16 * 128;
#define STAMP(i)
#define STAMP_FLUSH()
constexpr float LOG2E = 1.4426950408889634f;
constexpr float SC = 0.0625f * LOG2E;

__device__ __forceinline__ unsigned pk_bf16(float a, float b) {
  f32x2 x = {a, b};
  return __builtin_bit_cast(unsigned, __builtin_convertvector(x, bf16x2));
}
__device__ __forceinline__ float bf_lo(unsigned u) { return __builtin_bit_cast(float, u << 16); }
__device__ __forceinline__ float bf_hi(unsigned u) { return __builtin_bit_cast(float, u & 0xffff0000u); }
__device__ __forceinline__ float xmax(float x) {
  auto s = __builtin_amdgcn_permlane16_swap(__float_as_uint(x), __float_as_uint(x), false, false);
  x = fmaxf(__uint_as_float(s[0]), __uint_as_float(s[1]));
  auto t = __builtin_amdgcn_permlane32_swap(__float_as_uint(x), __float_as_uint(x), false, false);
  return fmaxf(__uint_as_float(t[0]), __uint_as_float(t[1]));
}
__device__ __forceinline__ void dma16(unsigned voff, const void* sbase, unsigned ldsaddr) {
  asm volatile("s_mov_b32 m0, %2\n\ts_nop 0\n\tglobal_load_lds_dwordx4 %0, %1 nt" ::"v"(voff), "s"(sbase), "s"(ldsaddr) : "memory");
}
template <int N> __device__ __forceinline__ void wait_vm() { asm volatile("s_waitcnt vmcnt(%0)" ::"i"(N) : "memory"); }
__device__ __forceinline__ void wait_lgkm0() { asm volatile("s_waitcnt lgkmcnt(0)" ::: "memory"); }
__device__ __forceinline__ void lds_wait_gt(unsigned addr, unsigned need, unsigned& seen) {
  unsigned tmp, cnt, sv = (unsigned)__builtin_amdgcn_readfirstlane(seen);
  asm volatile(
      "s_cmp_gt_u32 %0, %4\n\t"
      "s_cbranch_scc1 2f\n\t"
      "s_mov_b32 %2, 0x400000\n"
      "1:\n\t"
      "ds_read_b32 %1, %3\n\t"
      "s_waitcnt lgkmcnt(0)\n\t"
      "v_readfirstlane_b32 %0, %1\n\t"
      "s_cmp_gt_u32 %0, %4\n\t"
      "s_cbranch_scc1 2f\n\t"
      "s_sleep 1\n\t"
      "s_sub_u32 %2, %2, 1\n\t"
      "s_cbranch_scc0 1b\n"
      "2:"
      : "+&s"(sv), "=&v"(tmp), "=&s"(cnt)
      : "v"(addr), "s"(need)
      : "memory", "scc");
  seen = sv;
}
__device__ __forceinline__ unsigned lds_min4(unsigned addr) {
  u32x4 t;
  asm volatile("ds_read_b128 %0, %1\n\ts_waitcnt lgkmcnt(0)" : "=&v"(t) : "v"(addr) : "memory");
  return (unsigned)__builtin_amdgcn_readfirstlane(min(min(t[0], t[1]), min(t[2], t[3])));
}
}

__global__ __launch_bounds__(384, 2) void tree_attn_kernel(const unsigned short* __restrict__ q, const unsigned short* __restrict__ kc,
                                                           const unsigned short* __restrict__ vc, const unsigned short* __restrict__ kt,
                                                           const unsigned short* __restrict__ vt, const unsigned short* __restrict__ mask,
                                                           unsigned short* __restrict__ out, char* __restrict__ ws) {
  extern __shared__ __attribute__((aligned(16))) char lds_raw[];
  LAS char* lds = (LAS char*)lds_raw;
  const unsigned lds0 = (unsigned)(uintptr_t)lds;
  volatile LAS unsigned* ctl = (volatile LAS unsigned*)(lds + LDS_CTL);
  const unsigned ctla = lds0 + LDS_CTL;
  const int tid = threadIdx.x, lane = tid & 63;
  const int wave = __builtin_amdgcn_readfirstlane(tid >> 6);
  const int bid = blockIdx.x;
  const int kvh = bid & 7, split = (bid >> 3) & 3, b = bid >> 5;
  const int head = b * HKV + kvh;
  STAMP(0);

  if (__builtin_expect(wave == 0, 1)) {
    const char* kb = (const char*)(kc + ((size_t)head * NC + (size_t)split * KEYS) * ND);
    const char* vb = (const char*)(vc + ((size_t)head * NC + (size_t)split * KEYS) * ND);
    unsigned koff[8];
#pragma unroll
    for (int p = 0; p < 8; ++p) {
      const int row = 2 * p + (lane >> 5);
      koff[p] = row * 512 + (((lane & 31) ^ (row & 15)) << 4);
    }
    const unsigned voff = (lane >> 3) * 512 + (((lane & 7) ^ (((lane >> 4) & 3) << 1)) << 4);
    unsigned dst = lds0;
    int pub = 0;
#pragma unroll 1
    for (int st = 0; st < NST; ++st) {
      if (st >= RING) {
        const unsigned need = (unsigned)(st - RING + 1);
        if (lds_min4(ctla + 4 * C_DONE) < need) {
          wait_vm<0>();
          pub = st;
          ctl[C_FULL] = (unsigned)pub;
          for (int spins = 0; spins < (1 << 22); ++spins) {
            __builtin_amdgcn_s_sleep(1);
            if (lds_min4(ctla + 4 * C_DONE) >= need) break;
          }
        }
      }
      if (st < 16) {
        const char* src = kb + (size_t)st * SLOT;
#pragma unroll
        for (int p = 0; p < 16; ++p) dma16(koff[p & 7], src + (p >> 3) * 8192, dst + p * 1024);
      } else {
        const int blk = (split + 1 + ((st - 16) >> 2)) & 3;
        const char* src = vb + (size_t)(st & 3) * 65536 + blk * 128;
#pragma unroll
        for (int p = 0; p < 16; ++p) dma16(voff, src + p * 4096, dst + p * 1024);
      }
      dst += SLOT;
      if (dst == lds0 + RING * SLOT) dst = lds0;
      if (st == 0) __builtin_amdgcn_s_barrier();
      if (st + 1 - pub > INFL) {
        wait_vm<16 * INFL>();
        ++pub;
        ctl[C_FULL] = (unsigned)pub;
        STAMP(pub);
      }
    }
    if (INFL > 2 && NST - pub > 2) { wait_vm<32>(); pub = NST - 2; ctl[C_FULL] = (unsigned)pub; STAMP(30); }
    if (INFL > 1 && NST - pub > 1) { wait_vm<16>(); pub = NST - 1; ctl[C_FULL] = (unsigned)pub; STAMP(31); }
    wait_vm<0>();
    ctl[C_FULL] = (unsigned)NST;
    STAMP(32);
    STAMP_FLUSH();
    return;
  }

  if (wave == 1) {
    if (lane < 16) ctl[lane] = 0u;
    {
      const char* tk = (const char*)(kt + ((size_t)head * NT + split * 8) * ND);
      const char* tv = (const char*)(vt + ((size_t)head * NT + split * 8) * ND);
#pragma unroll
      for (int p = 0; p < 4; ++p) {
        const int row = 2 * p + (lane >> 5);
        dma16((unsigned)(row * 512 + (((lane & 31) ^ row) << 4)), tk, lds0 + LDS_TK + p * 1024);
        dma16((unsigned)(lane * 16 + p * 1024), tv, lds0 + LDS_TV + p * 1024);
      }
    }
    GAS u64* epoch_p = (GAS u64*)(ws + WS_EPOCH + (size_t)head * 128);
    const u64 tag = __hip_atomic_load(epoch_p, __ATOMIC_RELAXED, __HIP_MEMORY_SCOPE_AGENT) + 1ull;
    wait_lgkm0();
    __builtin_amdgcn_s_barrier();
    wait_vm<0>();
    ctl[C_INREADY] = 1u;
    const auto rsrc = __builtin_amdgcn_make_buffer_rsrc((void*)(ws + WS_MSG + (size_t)head * (16 * MSG_STRIDE)), 0, 16 * MSG_STRIDE, 0x00020000);
    GAS u64* flags = (GAS u64*)(ws + WS_FLAG + (size_t)head * (16 * 128));
    int so = 0, si = 0;
#pragma unroll 1
    for (int spins = 0; spins < (1 << 21) && si < 3; ++spins) {
      u32x4 w[8];
      if (so < 3 && lds_min4(ctla + 4 * C_OUTCNT) > (unsigned)so) {
        STAMP(1 + 4 * so);
#pragma unroll
        for (int k = 0; k < 8; ++k) w[k] = *(const LAS u32x4*)(lds + LDS_OUT + k * 1024 + lane * 16);
        const unsigned l = *(const LAS unsigned*)(lds + LDS_OUT + 8192 + lane * 4);
        const int blk = (split + 1 + so) & 3;
        const int off = (blk * 4 + split) * MSG_STRIDE;
#pragma unroll
        for (int k = 0; k < 8; ++k) __builtin_amdgcn_raw_buffer_store_b128(w[k], rsrc, off + k * 1024 + lane * 16, 0, 16);
        __builtin_amdgcn_raw_buffer_store_b32(l, rsrc, off + 8192 + lane * 4, 0, 16);
        wait_lgkm0();
        ++so;
        ctl[C_OUTFREE] = (unsigned)so;
        wait_vm<0>();
        if (lane == 0) __hip_atomic_store(flags + (blk * 4 + split) * 16, tag, __ATOMIC_RELAXED, __HIP_MEMORY_SCOPE_AGENT);
        STAMP(2 + 4 * (so - 1));
      } else if (si < so) {
        const int prod = (split + 3 - si) & 3;
        const u64 f = __hip_atomic_load(flags + (split * 4 + prod) * 16, __ATOMIC_RELAXED, __HIP_MEMORY_SCOPE_AGENT);
        if (__builtin_amdgcn_readfirstlane((unsigned)(f == tag))) {
          asm volatile("" ::: "memory");
          STAMP(3 + 4 * si);
          const int ioff = (split * 4 + prod) * MSG_STRIDE;
#pragma unroll
          for (int k = 0; k < 8; ++k) w[k] = __builtin_amdgcn_raw_buffer_load_b128(rsrc, ioff + k * 1024 + lane * 16, 0, 16);
          const unsigned li = __builtin_amdgcn_raw_buffer_load_b32(rsrc, ioff + 8192 + lane * 4, 0, 16);
#pragma unroll
          for (int k = 0; k < 8; ++k) *(LAS u32x4*)(lds + LDS_IN + si * MSG + k * 1024 + lane * 16) = w[k];
          *(LAS unsigned*)(lds + LDS_IN + si * MSG + 8192 + lane * 4) = li;
          wait_lgkm0();
          ++si;
          ctl[C_INREADY] = (unsigned)(si + 1);
          STAMP(4 + 4 * (si - 1));
        } else {
          __builtin_amdgcn_s_sleep(1);
        }
      } else {
        __builtin_amdgcn_s_sleep(2);
      }
    }
    if (split == 0 && lane == 0) __hip_atomic_store(epoch_p, tag, __ATOMIC_RELAXED, __HIP_MEMORY_SCOPE_AGENT);
    STAMP_FLUSH();
    return;
  }

  const int cw = wave - 2;
  const int r = lane & 15, g = lane >> 4;
  const size_t qrow = ((size_t)b * HQ + kvh * 2) * NT + cw * 16 + r;
  s16x8 qf[8];
#pragma unroll
  for (int ks = 0; ks < 8; ++ks) qf[ks] = *(const s16x8*)(q + qrow * ND + ks * 32 + g * 8);
  const int trow = (cw & 1) * 16 + r;
  const u32x2 mk = *(const u32x2*)(mask + ((size_t)b * NT + trow) * NT + split * 8 + (g & 1) * 4);
  __builtin_amdgcn_s_barrier();

  unsigned seen = 0, seen_in = 0;
  auto release = [&](int st) {
    wait_lgkm0();
    ctl[C_DONE + cw] = (unsigned)(st + 1);
  };

  f32x4 S[32];
  unsigned kaddr[4];
#pragma unroll
  for (int k4 = 0; k4 < 4; ++k4) kaddr[k4] = r * 512 + (((4 * k4 + g) ^ r) << 4);
  float tt[4];
  float mx;
  {
    lds_wait_gt(ctla + 4 * C_INREADY, 0u, seen_in);
    const int r7 = r & 7;
    s16x8 kf[8];
#pragma unroll
    for (int ks = 0; ks < 8; ++ks) kf[ks] = *(const LAS s16x8*)(lds + LDS_TK + r7 * 512 + (((4 * ks + g) ^ r7) << 4));
    __builtin_amdgcn_sched_barrier(0);
    f32x4 St = {0.f, 0.f, 0.f, 0.f};
#pragma unroll
    for (int ks = 0; ks < 8; ++ks) St = __builtin_amdgcn_mfma_f32_16x16x32_bf16(kf[ks], qf[ks], St, 0, 0, 0);
    const float mv[4] = {bf_lo(mk[0]), bf_hi(mk[0]), bf_lo(mk[1]), bf_hi(mk[1])};
#pragma unroll
    for (int i = 0; i < 4; ++i) {
      const float bias = (1.0f - mv[i]) * -1000000000.0f;
      tt[i] = g < 2 ? (St[i] * 0.0625f + bias) * LOG2E : -__builtin_inff();
    }
    mx = -__builtin_inff();
  }
#pragma unroll
  for (int st = 0; st < 16; ++st) {
    lds_wait_gt(ctla + 4 * C_FULL, (unsigned)st, seen);
    STAMP(1 + st);
    s16x8 kf[16];
#pragma unroll
    for (int t = 0; t < 2; ++t)
#pragma unroll
      for (int ks = 0; ks < 8; ++ks)
        kf[t * 8 + ks] = *(const LAS s16x8*)(lds + (st % RING) * SLOT + t * 8192 + (ks >> 2) * 256 + kaddr[ks & 3]);
    __builtin_amdgcn_sched_barrier(0);
    if (st > 0) {
#pragma unroll
      for (int t = 0; t < 2; ++t) mx = fmaxf(fmaxf(mx, fmaxf(S[2 * st - 2 + t][0], S[2 * st - 2 + t][1])), fmaxf(S[2 * st - 2 + t][2], S[2 * st - 2 + t][3]));
    }
    f32x4 a0 = {0.f, 0.f, 0.f, 0.f}, a1 = {0.f, 0.f, 0.f, 0.f};
#pragma unroll
    for (int ks = 0; ks < 8; ++ks) a0 = __builtin_amdgcn_mfma_f32_16x16x32_bf16(kf[ks], qf[ks], a0, 0, 0, 0);
    release(st);
#pragma unroll
    for (int ks = 0; ks < 8; ++ks) a1 = __builtin_amdgcn_mfma_f32_16x16x32_bf16(kf[8 + ks], qf[ks], a1, 0, 0, 0);
    S[2 * st] = a0;
    S[2 * st + 1] = a1;
  }
  STAMP(17);
#pragma unroll
  for (int t = 30; t < 32; ++t) mx = fmaxf(fmaxf(mx, fmaxf(S[t][0], S[t][1])), fmaxf(S[t][2], S[t][3]));
  mx *= SC;
  mx = fmaxf(fmaxf(mx, fmaxf(tt[0], tt[1])), fmaxf(tt[2], tt[3]));
  mx = xmax(mx);
  u32x4 P[16];
  const s16x8 ones8 = {0x3f80, 0x3f80, 0x3f80, 0x3f80, 0x3f80, 0x3f80, 0x3f80, 0x3f80};
  const s16x4 ones4 = {0x3f80, 0x3f80, 0x3f80, 0x3f80};
  const u32x2 Pt = {pk_bf16(__builtin_amdgcn_exp2f(tt[0] - mx), __builtin_amdgcn_exp2f(tt[1] - mx)),
                    pk_bf16(__builtin_amdgcn_exp2f(tt[2] - mx), __builtin_amdgcn_exp2f(tt[3] - mx))};
  f32x4 la = __builtin_amdgcn_mfma_f32_16x16x16bf16_1k(ones4, __builtin_bit_cast(s16x4, Pt), f32x4{0.f, 0.f, 0.f, 0.f}, 0, 0, 0);
#pragma unroll
  for (int u = 0; u < 16; ++u) {
    float e[8];
#pragma unroll
    for (int i = 0; i < 8; ++i) e[i] = __builtin_amdgcn_exp2f(fmaf(S[2 * u + (i >> 2)][i & 3], SC, -mx));
    P[u] = u32x4{pk_bf16(e[0], e[1]), pk_bf16(e[2], e[3]), pk_bf16(e[4], e[5]), pk_bf16(e[6], e[7])};
    la = __builtin_amdgcn_mfma_f32_16x16x32_bf16(ones8, __builtin_bit_cast(s16x8, P[u]), la, 0, 0, 0);
  }
  const float lsum = la[0];
  const float rl = 1.0f / lsum;
  const float lse = mx + __builtin_amdgcn_logf(lsum);
  if (g == 0) *(LAS float*)(lds + LDS_OUT + 8192 + (cw * 16 + r) * 4) = lse;
  STAMP(18);

  const int qq = (lane & 15) >> 2, pp = lane & 3;
  unsigned vaddr[4];
  {
    const int x = (2 * g + (qq >> 1)) & 3;
#pragma unroll
    for (int t = 0; t < 4; ++t) vaddr[t] = (4 * g + qq) * 128 + ((t ^ x) << 5) + pp * 8;
  }
  const unsigned tvaddr = LDS_TV + ((4 * g + qq) & 7) * 512 + pp * 8;
  f32x4 O[4];
  unsigned sbase = (16 % RING) * SLOT, seen_free = 0;
#pragma unroll 1
  for (int jj = 0; jj < 4; ++jj) {
    const int blk = (split + 1 + jj) & 3;
#pragma unroll
    for (int t = 0; t < 4; ++t) {
      const s16x4 v4 = __builtin_amdgcn_ds_read_tr16_b64_v4i16((LAS s16x4*)(lds + tvaddr + blk * 128 + t * 32));
      O[t] = __builtin_amdgcn_mfma_f32_16x16x16bf16_1k(v4, __builtin_bit_cast(s16x4, Pt), f32x4{0.f, 0.f, 0.f, 0.f}, 0, 0, 0);
    }
#pragma unroll
    for (int i = 0; i < 4; ++i) {
      const int st = 16 + jj * 4 + i;
      lds_wait_gt(ctla + 4 * C_FULL, (unsigned)st, seen);
      STAMP(3 + st);
      s16x4 vf[4][4][2];
#pragma unroll
      for (int u = 0; u < 4; ++u)
#pragma unroll
        for (int t = 0; t < 4; ++t)
#pragma unroll
          for (int h = 0; h < 2; ++h)
            vf[u][t][h] = __builtin_amdgcn_ds_read_tr16_b64_v4i16((LAS s16x4*)(lds + sbase + vaddr[t] + u * 4096 + h * 2048));
      __builtin_amdgcn_sched_barrier(0);
#pragma unroll
      for (int u = 0; u < 4; ++u) {
        if (u == 3) release(st);
#pragma unroll
        for (int t = 0; t < 4; ++t)
          O[t] = __builtin_amdgcn_mfma_f32_16x16x32_bf16(__builtin_shufflevector(vf[u][t][0], vf[u][t][1], 0, 1, 2, 3, 4, 5, 6, 7),
                                                         __builtin_bit_cast(s16x8, P[4 * i + u]), O[t], 0, 0, 0);
      }
      sbase += SLOT;
      if (sbase == RING * SLOT) sbase = 0;
    }
    if (jj < 3) {
      const u32x4 w0 = {pk_bf16(O[0][0] * rl, O[0][1] * rl), pk_bf16(O[0][2] * rl, O[0][3] * rl), pk_bf16(O[1][0] * rl, O[1][1] * rl), pk_bf16(O[1][2] * rl, O[1][3] * rl)};
      const u32x4 w1 = {pk_bf16(O[2][0] * rl, O[2][1] * rl), pk_bf16(O[2][2] * rl, O[2][3] * rl), pk_bf16(O[3][0] * rl, O[3][1] * rl), pk_bf16(O[3][2] * rl, O[3][3] * rl)};
      if (jj > 0) lds_wait_gt(ctla + 4 * C_OUTFREE, (unsigned)(jj - 1), seen_free);
      *(LAS u32x4*)(lds + LDS_OUT + cw * 2048 + lane * 16) = w0;
      *(LAS u32x4*)(lds + LDS_OUT + cw * 2048 + 1024 + lane * 16) = w1;
      wait_lgkm0();
      ctl[C_OUTCNT + cw] = (unsigned)(jj + 1);
    }
  }
  STAMP(36);
  lds_wait_gt(ctla + 4 * C_INREADY, 3u, seen_in);
  STAMP(37);
  u32x4 X[3][2];
  float xl[3];
#pragma unroll
  for (int p = 0; p < 3; ++p) {
    X[p][0] = *(const LAS u32x4*)(lds + LDS_IN + p * MSG + cw * 2048 + lane * 16);
    X[p][1] = *(const LAS u32x4*)(lds + LDS_IN + p * MSG + cw * 2048 + 1024 + lane * 16);
    xl[p] = *(const LAS float*)(lds + LDS_IN + p * MSG + 8192 + (cw * 16 + r) * 4);
  }
  const float M = fmaxf(fmaxf(lse, xl[0]), fmaxf(xl[1], xl[2]));
  float w0 = __builtin_amdgcn_exp2f(lse - M), w1 = __builtin_amdgcn_exp2f(xl[0] - M), w2 = __builtin_amdgcn_exp2f(xl[1] - M), w3 = __builtin_amdgcn_exp2f(xl[2] - M);
  const float inv = 1.0f / (w0 + w1 + w2 + w3);
  w0 *= inv * rl; w1 *= inv; w2 *= inv; w3 *= inv;
  unsigned short* orow = out + qrow * ND + split * 64 + g * 4;
#pragma unroll
  for (int t = 0; t < 4; ++t) {
    const unsigned a0 = X[0][t >> 1][(t & 1) * 2], a1 = X[0][t >> 1][(t & 1) * 2 + 1];
    const unsigned b0 = X[1][t >> 1][(t & 1) * 2], b1 = X[1][t >> 1][(t & 1) * 2 + 1];
    const unsigned c0 = X[2][t >> 1][(t & 1) * 2], c1 = X[2][t >> 1][(t & 1) * 2 + 1];
    const float o0 = O[t][0] * w0 + bf_lo(a0) * w1 + bf_lo(b0) * w2 + bf_lo(c0) * w3;
    const float o1 = O[t][1] * w0 + bf_hi(a0) * w1 + bf_hi(b0) * w2 + bf_hi(c0) * w3;
    const float o2 = O[t][2] * w0 + bf_lo(a1) * w1 + bf_lo(b1) * w2 + bf_lo(c1) * w3;
    const float o3 = O[t][3] * w0 + bf_hi(a1) * w1 + bf_hi(b1) * w2 + bf_hi(c1) * w3;
    *(u32x2*)(orow + t * 16) = u32x2{pk_bf16(o0, o1), pk_bf16(o2, o3)};
  }
  STAMP(38);
  STAMP_FLUSH();
}

extern "C" void kernel_launch(void* const* d_in, const int* in_sizes, int n_in, void* d_out, int out_size, void* d_ws, size_t ws_size,
                              hipStream_t stream) {
  (void)in_sizes; (void)n_in; (void)out_size; (void)ws_size;
  (void)hipFuncSetAttribute((const void*)tree_attn_kernel, hipFuncAttributeMaxDynamicSharedMemorySize, LDS_BYTES);
  tree_attn_kernel<<<256, 384, LDS_BYTES, stream>>>((const unsigned short*)d_in[0], (const unsigned short*)d_in[1], (const unsigned short*)d_in[2],
                                                    (const unsigned short*)d_in[3], (const unsigned short*)d_in[4], (const unsigned short*)d_in[5],
                                                    (unsigned short*)d_out, (char*)d_ws);
}
```

```cpp
#include <hip/hip_runtime.h>
#include <type_traits>

typedef __attribute__((ext_vector_type(16))) unsigned u32x16;
typedef __attribute__((ext_vector_type(4))) unsigned u32x4;
typedef unsigned long long u64;
#define LAS __attribute__((address_space(3)))
#define GAS __attribute__((address_space(1)))

namespace {
constexpr int HQ = 16, HKV = 8, NT = 32;
constexpr int T0 = 60;
constexpr int TA = 49;
constexpr int GT = 40;
constexpr int TB = 14;
constexpr int J0 = 20, JU = 3;
constexpr int XU = 2;
constexpr int SPINI = 0x400000;
constexpr int UNIT = 4096;
constexpr int KP = 1056;
constexpr int KT = 16 * KP;
constexpr int SLOT = KT + UNIT;
constexpr int LDS_T = 5 * SLOT;
constexpr int LDS_O = LDS_T + 12800;
constexpr int LDS_P = LDS_O + 3 * UNIT;
constexpr int LDS_M = LDS_P + 31 * 1024;
constexpr int LDS_CTL = LDS_P + 32768;
constexpr int LDS_E = LDS_CTL + 384;
constexpr int LDS_BYTES = LDS_E + 256;
constexpr int NP = 814;
enum { C_DONE = 4, C_FULL = 15 };
constexpr int WS_HEAD = 65536;
constexpr int WS_MSG = 4096;
constexpr int MSG_STRIDE = 9216;

template <int N> __device__ __forceinline__ void wait_vm() { asm volatile("s_waitcnt vmcnt(%0)" ::"i"(N) : "memory"); }
#define REPORT_ASM                                   \
  "s_getreg_b32 %[x], hwreg(HW_REG_IB_STS)\n\t"      \
  "s_bfe_u32 %[y], %[x], 0x20016\n\t"                \
  "s_and_b32 %[x], %[x], 15\n\t"                     \
  "s_lshl4_add_u32 %[x], %[y], %[x]\n\t"             \
  "v_subrev_u32 %[t], %[x], %[q]\n\t"                \
  "ds_write_b32 %[f], %[t]"
#define UNIT_ASM                                                 \
  "global_load_lds_dwordx4 %[a], %[s] nt\n\t"                    \
  "global_load_lds_dwordx4 %[b], %[s] offset:%[o1] nt\n\t"       \
  "global_load_lds_dwordx4 %[c], %[s] offset:%[o2] nt\n\t"       \
  "global_load_lds_dwordx4 %[d], %[s] offset:%[o3] nt\n\t"
#define UNIT_IN [dst] "s"(dst), [off] "i"(OFF), [a] "v"(v[0]), [b] "v"(v[1]), [c] "v"(v[2]), [d] "v"(v[3]), [s] "s"(sbase), [o1] "i"(STEP), [o2] "i"(2 * STEP), [o3] "i"(3 * STEP)
template <int OFF, int STEP>
__device__ __forceinline__ void dma_unit(const unsigned* v, const void* sbase, unsigned dst, int& cnt, unsigned faddr) {
  unsigned x, y, t;
  asm volatile("s_add_u32 m0, %[dst], %[off]\n\tv_add_u32 %[q], 4, %[q]\n\t" UNIT_ASM REPORT_ASM
               : [q] "+v"(cnt), [x] "=&s"(x), [y] "=&s"(y), [t] "=&v"(t)
               : UNIT_IN, [f] "v"(faddr)
               : "memory", "scc");
}
template <int OFF, int STEP>
__device__ __forceinline__ void dma_unit_p(const unsigned* v, const void* sbase, unsigned dst, int& cnt, unsigned faddr, int& got, unsigned daddr, unsigned due, int& lag, unsigned& z) {
  unsigned x, y, t, n;
  asm volatile(
      "s_memrealtime s[98:99]\n\t"
      "ds_read_b32 %[g], %[da]\n\t"
      "s_add_u32 m0, %[dst], %[off]\n\t"
      "v_add_u32 %[q], 4, %[q]\n\t" UNIT_ASM
      "s_mov_b32 %[z], -1\n\t"
      "s_movk_i32 %[n], 0x400\n"
      "1:\n\t"
      "s_getreg_b32 %[x], hwreg(HW_REG_IB_STS)\n\t"
      "s_and_b32 %[x], %[x], 0xc0000f\n\t"
      "s_cmp_eq_u32 %[x], %[z]\n\t"
      "s_cbranch_scc1 2f\n\t"
      "s_mov_b32 %[z], %[x]\n\t"
      "s_lshr_b32 %[y], %[x], 18\n\t"
      "s_and_b32 %[x], %[x], 15\n\t"
      "s_or_b32 %[x], %[x], %[y]\n\t"
      "v_subrev_u32 %[t], %[x], %[q]\n\t"
      "ds_write_b32 %[f], %[t]\n"
      "2:\n\t"
      "s_waitcnt lgkmcnt(0)\n\t"
      "s_sub_u32 %[l], s98, %[due]\n\t"
      "s_cmp_ge_i32 %[l], 0\n\t"
      "s_cbranch_scc1 3f\n\t"
      "s_memrealtime s[98:99]\n\t"
      "s_sub_u32 %[n], %[n], 1\n\t"
      "s_cbranch_scc0 1b\n\t"
      "s_waitcnt lgkmcnt(0)\n"
      "3:"
      : [q] "+v"(cnt), [g] "=&v"(got), [x] "=&s"(x), [y] "=&s"(y), [t] "=&v"(t), [z] "=&s"(z), [n] "=&s"(n), [l] "=&s"(lag)
      : UNIT_IN, [f] "v"(faddr), [da] "v"(daddr), [due] "s"(due)
      : "memory", "scc", "s98", "s99");
}
template <int OFF, int STEP>
__device__ __forceinline__ void dma_unit0(const unsigned* v, const void* sbase, unsigned dst) {
  asm volatile("s_add_u32 m0, %[dst], %[off]\n\ts_nop 0\n\t" UNIT_ASM : : UNIT_IN : "memory", "scc");
}
template <int OFF>
__device__ __forceinline__ void dma_half(const unsigned* v, const void* sbase, unsigned dst) {
  asm volatile(
      "s_add_u32 m0, %[dst], %[off]\n\t"
      "s_nop 0\n\t"
      "global_load_lds_dwordx4 %[a], %[s] nt\n\t"
      "global_load_lds_dwordx4 %[b], %[s] offset:1024 nt"
      :
      : [dst] "s"(dst), [off] "i"(OFF), [a] "v"(v[0]), [b] "v"(v[1]), [s] "s"(sbase)
      : "memory", "scc");
}
__device__ __forceinline__ void look(int cnt, unsigned faddr, int& got, unsigned daddr) {
  unsigned x, y, t;
  asm volatile("ds_read_b32 %[g], %[da]\n\t" REPORT_ASM "\n\ts_waitcnt lgkmcnt(0)"
               : [g] "=&v"(got), [x] "=&s"(x), [y] "=&s"(y), [t] "=&v"(t)
               : [q] "v"(cnt), [f] "v"(faddr), [da] "v"(daddr)
               : "memory", "scc");
}
template <int N, int K, int E> __device__ __forceinline__ void drain(unsigned addr) {
  if constexpr (K >= E) {
    wait_vm<K>();
    asm volatile("ds_max_i32 %0, %1" ::"v"(addr), "v"(N - K) : "memory");
    drain<N, K - 4, E>(addr);
  }
}
#define SB() __builtin_amdgcn_sched_barrier(0)
}

extern "C" __global__ __launch_bounds__(320, 2) void tree_attn_kernel(const char* __restrict__ kc, const char* __restrict__ vc, const char* __restrict__ kt,
                                                                      const char* __restrict__ vt, const char* __restrict__ q, const char* __restrict__ mask,
                                                                      char* __restrict__ ws, char* __restrict__ out) {
  extern __shared__ __attribute__((aligned(16))) char lds_raw[];
  LAS char* lds = (LAS char*)lds_raw;
  const unsigned lds0 = (unsigned)(uintptr_t)lds;
  const unsigned ctla = lds0 + LDS_CTL;
  const int tid = threadIdx.x, lane = tid & 63;
  const int wave = __builtin_amdgcn_readfirstlane(tid >> 6);
  const int bid = blockIdx.x;
  const int b = bid & 7, cs = (bid >> 3) & 1, ks = (bid >> 4) & 1, kvh = bid >> 5;
  const int head = b * HKV + kvh;
  const int sb = (2 * cs + 1 - ks) * 128, ob = (2 * cs + ks) * 128;
  asm volatile(
      "s_cmp_lg_u32 %[w], 2\n\t"
      "s_cbranch_scc1 1f\n\t"
      "s_getpc_b64 s[98:99]\n"
      "2:\n\t"
      "v_and_or_b32 v255, %[t], 7, %[b]\n\t"
      "v_lshlrev_b32 v255, 6, v255\n\t"
      "v_min_u32 v255, .Lta_end-2b-4, v255\n\t"
      "global_load_dword v255, v255, s[98:99]\n"
      "1:"
      :
      : [w] "s"(wave), [t] "v"(tid), [b] "s"(bid & ~7)
      : "scc", "s98", "s99", "v255");

  if (__builtin_expect(wave == 0, 1)) {
    u32x16 ka;
    u64 t64;
    asm volatile("s_load_dwordx16 %0, %2, 0x0\n\ts_memrealtime %1" : "=&s"(ka), "=&s"(t64) : "s"(__builtin_amdgcn_kernarg_segment_ptr()));
    SB();
    auto arg = [&](int i) { return (const char*)(const GAS char*)(((u64)ka[2 * i + 1] << 32) | ka[2 * i]); };
    __builtin_amdgcn_s_setprio(3);
    int lane = tid;
    asm volatile("" : "+v"(lane));
    constexpr unsigned TS = 32768u;
    unsigned koff[16];
#pragma unroll
    for (int p = 0; p < 16; ++p) koff[p] = 128 + p * 1024 - (p & 3) * KP + ((lane ^ (lane >> 5)) << 4);
    unsigned voff[4];
#pragma unroll
    for (int p = 0; p < 4; ++p) voff[p] = p * 3072 + (lane >> 3) * 512 + (((lane & 7) ^ (((lane >> 4) & 3) << 1)) << 4);
    const unsigned faddr = ctla + 4 * (C_FULL + lane);
    const unsigned daddr = ctla + 4 * (C_DONE + (lane & 3));
    int got = -3;
    int qc = 48;
    *(volatile LAS int*)(lds + LDS_CTL + 4 * lane) = (lane < 8) ? -3 : 0;
    unsigned ko = head * 1048576u + ks * 16384u, qo = head * 32768u;
#pragma unroll
    for (int p = 0; p < 16; ++p) asm volatile("" : "+v"(koff[p]));
#pragma unroll
    for (int p = 0; p < 4; ++p) asm volatile("" : "+v"(voff[p]));
    asm volatile("" : "+s"(ko), "+s"(qo));
    SB();
    asm volatile("s_waitcnt lgkmcnt(0)" : "+s"(ka), "+s"(t64));
    SB();
    const char* ksrc = arg(0) + ko - 128;
    const char* qsrc = arg(4) + qo - 128;
    auto ktile = [&](const char* src, auto off_) {
      constexpr int off = decltype(off_)::value;
      dma_unit0<off, KP>(koff, src, lds0);
      dma_unit0<off + 4 * KP, KP>(koff + 4, src, lds0);
      dma_unit0<off + 8 * KP, KP>(koff + 8, src, lds0);
      dma_unit0<off + 12 * KP, KP>(koff + 12, src, lds0);
    };
    ktile(qsrc, std::integral_constant<int, 3 * SLOT>{});
    ktile(qsrc + 16384, std::integral_constant<int, 4 * SLOT>{});
    ktile(ksrc, std::integral_constant<int, 0>{});
    __builtin_amdgcn_s_barrier();
    const char* vsrc = arg(1) + ko;
    auto extras = [&]() {
      const unsigned to = head * 16384u + ks * 8192u;
      const char* tk = arg(2) + to - 128;
      const char* tv = arg(3) + to;
      dma_unit<LDS_T, KP>(koff, tk, lds0, qc, faddr);
      dma_unit<LDS_T + 4 * KP, KP>(koff + 4, tk, lds0, qc, faddr);
      dma_half<LDS_T + 8 * KP>(voff, tv + sb, lds0);
      dma_half<LDS_T + 8 * KP + 2048>(voff, tv + ob, lds0);
      asm volatile(
          "s_add_u32 m0, %[dst], %[om]\n\t"
          "v_add_u32 %[q], 6, %[q]\n\t"
          "global_load_lds_dwordx4 %[a], %[m] nt\n\t"
          "s_add_u32 m0, %[dst], %[oe]\n\t"
          "s_nop 0\n\t"
          "global_load_lds_dword %[z], %[e] nt"
          : [q] "+v"(qc)
          : [dst] "s"(lds0), [om] "i"(LDS_M), [oe] "i"(LDS_E), [a] "v"((lane >> 1) * 64 + (lane & 1) * 16), [z] "v"(0), [m] "s"(arg(5) + b * 2048 + ks * 32),
            [e] "s"(arg(6) + head * WS_HEAD + cs * 128)
          : "memory", "scc");
    };
    unsigned due = (unsigned)t64 + T0;
    auto room = [&](int want) {
      if (__builtin_expect(__builtin_amdgcn_ballot_w64(got < want) != 0ull, 0)) {
#pragma unroll 1
        for (int spins = 0; spins < SPINI; ++spins) {
          look(qc, faddr, got, daddr);
          if (__builtin_amdgcn_ballot_w64(got < want) == 0ull) break;
          __builtin_amdgcn_s_sleep(1);
        }
      }
    };
    int lag;
    unsigned opn;
    unsigned dst = lds0, own = lds0 + LDS_O;
    auto body = [&](int u, auto both_) {
      constexpr bool both = decltype(both_)::value;
      due += TA;
      ksrc += TS;
      const unsigned dv = dst;
      dst += SLOT;
      if (dst == lds0 + 5 * SLOT) dst = lds0;
      if (both) {
        dma_unit<0, 1024>(voff, vsrc + ob, own, qc, faddr);
        own += UNIT;
        if (own == lds0 + LDS_O + 3 * UNIT) own = lds0 + LDS_O;
      }
      dma_unit_p<KT, 1024>(voff, vsrc + sb, dv, qc, faddr, got, daddr, due, lag, opn);
      vsrc += TS;
      if (!both && u == XU) { extras(); due += GT; }
      {
        int lim;
        asm volatile("s_cmp_gt_i32 %[u], %[ju]\n\ts_cselect_b32 %[lim], 9999, %[j0]\n\ts_cmp_gt_i32 %[lag], %[lim]\n\ts_cselect_b32 %[lag], %[lag], 0\n\ts_add_u32 %[due], %[due], %[lag]"
                     : [due] "+s"(due), [lag] "+s"(lag), [lim] "=&s"(lim) : [j0] "i"(J0), [ju] "i"(JU), [u] "s"(u) : "scc");
      }
      room(u - 5);
      dma_unit<0, KP>(koff, ksrc, dst, qc, faddr);
      dma_unit<4 * KP, KP>(koff + 4, ksrc, dst, qc, faddr);
      dma_unit<8 * KP, KP>(koff + 8, ksrc, dst, qc, faddr);
      dma_unit<12 * KP, KP>(koff + 12, ksrc, dst, qc, faddr);
    };
#pragma unroll 1
    for (int u = 1;; u += 2) {
      body(u, std::true_type{});
      if (u == 31) break;
      body(u + 1, std::false_type{});
    }
    due += TA;
    dma_unit_p<KT, 1024>(voff, vsrc + sb, dst, qc, faddr, got, daddr, due, lag, opn);
    vsrc += ob - 30 * (int)TS;
    dst = lds0 + 2 * SLOT;
#pragma unroll 1
    for (int t = 27; t < 30; ++t) {
      room(t);
#pragma unroll 1
      for (int x = 0; x < 5; ++x) {
        due += TB;
        dma_unit_p<0, 1024>(voff, vsrc, dst, qc, faddr, got, daddr, due, lag, opn);
        vsrc += 2 * TS;
        dst += UNIT;
      }
    }
    dma_unit0<0, 1024>(voff, vsrc, dst);
    drain<NP, 44, 0>(faddr);
    asm volatile("s_endpgm");
  }

  const int cw = wave - 1, par = cw & 1, hd = cw >> 1;
  const int r = lane & 15, g = lane >> 4;
  const unsigned row0 = (b * HQ + kvh * 2) * NT + cw * 16;
  const unsigned slot = lds0 + (1 - par) * SLOT;
  const unsigned kpos = (r >> 1) * KP + (r & 1) * 512 + ((g ^ (r & 1)) << 4);
  const unsigned kaddr = slot + kpos, qaddr = lds0 + (3 + hd) * SLOT + kpos;
  const unsigned vaddr = slot + KT + (4 * g + (r >> 2)) * 128 + (((2 * g + (r >> 3)) & 3) << 5) + (lane & 3) * 8;
  const u64 pm = (u64)(ws + (head * WS_HEAD + WS_MSG + cs * (2 * MSG_STRIDE))), po = (u64)(out + row0 * 512 + ob);
  const u32x4 rmsg = {(unsigned)pm, (unsigned)(pm >> 32) & 0xffffu, 2 * MSG_STRIDE, 0x00020000u};
  const u32x4 rout = {(unsigned)po, (unsigned)(po >> 32) & 0xffffu, 16 * 512, 0x00020000u};
  const unsigned hb = lds0 + hd * 9216;
  u32x4 xv = {qaddr + par * 8 * KP, qaddr + (1 - par) * 8 * KP, lds0 + LDS_M + r * 32 + g * 8, 0u};
  u32x16 mv = {0xff800000u, 0u, 0xff800000u, 0u, kaddr, vaddr, vaddr ^ 32u, vaddr ^ 64u, vaddr ^ 96u, lds0 + LDS_P + hd * 16384 + lane * 16, 2u - par,
               lds0 + LDS_T + 2560 + hd * 5120 + lane * 16, ctla + 4 * (C_DONE + cw), 0u, hb + lane * 16, 0u};
  asm volatile(
      "s_barrier\n"
      "s_mov_b32 s50, 0x3db8aa3b\n"
      "s_mov_b32 s52, 0x40004000\n"
      "s_mov_b32 s53, 0x3f803f80\n"
      "s_mov_b32 s99, %[par]\n"
      "s_cmp_eq_u32 s99, 0\n"
      "s_cselect_b32 s41, 72, 48\n"
      "s_cselect_b32 s42, 76, 56\n"
      "s_cselect_b32 s43, 20992, 0\n"
      "s_movk_i32 s45, 150\n"
      "s_cmovk_i32 s45, 790\n"
      "s_mov_b32 s46, 0\n"
      "s_mov_b32 s47, 100864\n"
      "s_mov_b32 s55, 58\n"
      "v_mov_b64 v[64:65], 0\n"
      "v_mov_b64 v[66:67], 0\n"
      "v_mov_b64 v[68:69], 0\n"
      "v_mov_b64 v[70:71], 0\n"
      "v_mov_b64 v[72:73], 0\n"
      "v_mov_b64 v[74:75], 0\n"
      "v_mov_b64 v[76:77], 0\n"
      "v_mov_b64 v[78:79], 0\n"
      "v_mov_b64 v[80:81], 0\n"
      "v_mov_b64 v[82:83], 0\n"
      "v_mov_b64 v[84:85], 0\n"
      "v_mov_b64 v[86:87], 0\n"
      "v_mov_b64 v[88:89], 0\n"
      "v_mov_b64 v[90:91], 0\n"
      "v_mov_b64 v[92:93], 0\n"
      "v_mov_b64 v[94:95], 0\n"
      "v_mov_b64 v[192:193], 0\n"
      "v_mov_b64 v[194:195], 0\n"
      "v_mov_b64 v[196:197], 0\n"
      "v_mov_b64 v[198:199], 0\n"
      "v_mov_b64 v[200:201], 0\n"
      "v_mov_b64 v[202:203], 0\n"
      "v_mov_b64 v[204:205], 0\n"
      "v_mov_b64 v[206:207], 0\n"
      "v_mov_b64 v[208:209], 0\n"
      "v_mov_b64 v[210:211], 0\n"
      "v_mov_b64 v[212:213], 0\n"
      "v_mov_b64 v[214:215], 0\n"
      "v_mov_b64 v[216:217], 0\n"
      "v_mov_b64 v[218:219], 0\n"
      "v_mov_b64 v[220:221], 0\n"
      "v_mov_b64 v[222:223], 0\n"
      "v_subrev_u32 v160, 2, v186\n"
      "v_mov_b32 v244, v170\n"
      "s_mov_b32 s49, 0x400000\n"
      "s_branch .Lp1f_%=\n"
      ".Lp1_%=:\n"
      "s_sleep 1\n"
      ".Lp1f_%=:\n"
      "ds_read_b32 v189, v188 offset:64\n"
      "s_sub_u32 s49, s49, 1\n"
      "s_waitcnt lgkmcnt(0)\n"
      "v_readfirstlane_b32 s40, v189\n"
      "s_cbranch_scc1 .Lp1e_%=\n"
      "s_cmp_ge_i32 s40, %[qn]\n"
      "s_cbranch_scc0 .Lp1_%=\n"
      ".Lp1e_%=:\n"
      "ds_read_b128 v[0:3], v168 offset:0\n"
      "ds_read_b128 v[4:7], v168 offset:64\n"
      "ds_read_b128 v[8:11], v168 offset:128\n"
      "ds_read_b128 v[12:15], v168 offset:192\n"
      "ds_read_b128 v[16:19], v168 offset:256\n"
      "ds_read_b128 v[20:23], v168 offset:320\n"
      "ds_read_b128 v[24:27], v168 offset:384\n"
      "ds_read_b128 v[28:31], v168 offset:448\n"
      "ds_read_b128 v[32:35], v169 offset:0\n"
      "ds_read_b128 v[36:39], v169 offset:64\n"
      "ds_read_b128 v[40:43], v169 offset:128\n"
      "ds_read_b128 v[44:47], v169 offset:192\n"
      "ds_read_b128 v[48:51], v169 offset:256\n"
      "ds_read_b128 v[52:55], v169 offset:320\n"
      "ds_read_b128 v[56:59], v169 offset:384\n"
      "ds_read_b128 v[60:63], v169 offset:448\n"
      "ds_write_b32 v188, v160\n"
      "s_mov_b32 s58, 0x9e37b5c9\n"
      "s_mov_b32 s59, 0xc2b3e6ef\n"
      "s_mov_b32 s60, 0x85ebf4d7\n"
      "s_mov_b32 s61, 0x27d5c95b\n"
      "s_mov_b32 s62, 0x7f4bd3a5\n"
      "s_mov_b32 s63, 0x5bd1a98f\n"
      "s_mov_b32 s64, 0xb5299463\n"
      "s_mov_b32 s65, 0x68e3dd31\n"
      "s_mov_b32 s66, 0xd6e94f1d\n"
      "s_mov_b32 s67, 0x1b87a3ad\n"
      "s_cmp_eq_u32 s99, 0\n"
      "s_cbranch_scc1 .LT0_%=\n"
      ".LT1_%=:\n"
      "s_sub_u32 s54, s41, 8\n"
      "s_cmp_ge_i32 s40, s54\n"
      "s_cbranch_scc1 .Lp2e_%=\n"
      "s_mov_b32 s49, 0x400000\n"
      "s_branch .Lp2f_%=\n"
      ".Lp2_%=:\n"
      "s_sleep 1\n"
      ".Lp2f_%=:\n"
      "ds_read_b32 v189, v188 offset:64\n"
      "s_sub_u32 s49, s49, 1\n"
      "s_waitcnt lgkmcnt(0)\n"
      "v_readfirstlane_b32 s40, v189\n"
      "s_cbranch_scc1 .Lp2e_%=\n"
      "s_cmp_ge_i32 s40, s54\n"
      "s_cbranch_scc0 .Lp2_%=\n"
      ".Lp2e_%=:\n"
      "ds_read_b128 v[96:99], v180 offset:0\n"
      "ds_read_b128 v[100:103], v180 offset:64\n"
      "ds_read_b128 v[104:107], v180 offset:128\n"
      "ds_read_b128 v[108:111], v180 offset:192\n"
      "ds_read_b128 v[112:115], v180 offset:256\n"
      "ds_read_b128 v[116:119], v180 offset:320\n"
      "ds_read_b128 v[120:123], v180 offset:384\n"
      "ds_read_b128 v[124:127], v180 offset:448\n"
      "s_cmp_ge_i32 s40, s41\n"
      "s_cbranch_scc1 .Lp3e_%=\n"
      "s_mov_b32 s49, 0x400000\n"
      "s_branch .Lp3f_%=\n"
      ".Lp3_%=:\n"
      "s_sleep 1\n"
      ".Lp3f_%=:\n"
      "ds_read_b32 v189, v188 offset:64\n"
      "s_sub_u32 s49, s49, 1\n"
      "s_waitcnt lgkmcnt(0)\n"
      "v_readfirstlane_b32 s40, v189\n"
      "s_cbranch_scc1 .Lp3e_%=\n"
      "s_cmp_ge_i32 s40, s41\n"
      "s_cbranch_scc0 .Lp3_%=\n"
      ".Lp3e_%=:\n"
      "s_waitcnt lgkmcnt(7)\n"
      "v_mfma_f32_16x16x32_bf16 v[144:147], v[96:99], v[0:3], 0\n"
      "s_add_u32 s54, s43, 41984\n"
      "s_cmp_ge_u32 s54, 104960\n"
      "s_movk_i32 s44, 0xa4\n"
      "v_mfma_f32_16x16x32_bf16 v[148:151], v[96:99], v[32:35], 0\n"
      "ds_read_b128 v[96:99], v180 offset:8448\n"
      "s_waitcnt lgkmcnt(7)\n"
      "v_mfma_f32_16x16x32_bf16 v[144:147], v[100:103], v[4:7], v[144:147]\n"
      "s_cmovk_i32 s44, 0xff0a\n"
      "s_lshl_b32 s44, s44, 8\n"
      "s_sub_u32 s48, s47, s43\n"
      "v_mfma_f32_16x16x32_bf16 v[148:151], v[100:103], v[36:39], v[148:151]\n"
      "ds_read_b128 v[100:103], v180 offset:8512\n"
      "s_waitcnt lgkmcnt(7)\n"
      "v_mfma_f32_16x16x32_bf16 v[144:147], v[104:107], v[8:11], v[144:147]\n"
      "s_add_u32 s43, s43, s44\n"
      "s_sub_u32 s96, s43, s47\n"
      "s_add_u32 s47, s47, 4096\n"
      "v_mfma_f32_16x16x32_bf16 v[148:151], v[104:107], v[40:43], v[148:151]\n"
      "ds_read_b128 v[104:107], v180 offset:8576\n"
      "s_waitcnt lgkmcnt(7)\n"
      "v_mfma_f32_16x16x32_bf16 v[144:147], v[108:111], v[12:15], v[144:147]\n"
      "s_cmp_eq_u32 s47, 113152\n"
      "s_cselect_b32 s47, 100864, s47\n"
      "v_mfma_f32_16x16x32_bf16 v[148:151], v[108:111], v[44:47], v[148:151]\n"
      "ds_read_b128 v[108:111], v180 offset:8640\n"
      "s_waitcnt lgkmcnt(7)\n"
      "v_mfma_f32_16x16x32_bf16 v[144:147], v[112:115], v[16:19], v[144:147]\n"
      "v_mfma_f32_16x16x32_bf16 v[148:151], v[112:115], v[48:51], v[148:151]\n"
      "ds_read_b128 v[112:115], v180 offset:8704\n"
      "ds_read_b32 v189, v188 offset:64\n"
      "s_waitcnt lgkmcnt(8)\n"
      "v_mfma_f32_16x16x32_bf16 v[144:147], v[116:119], v[20:23], v[144:147]\n"
      "v_mfma_f32_16x16x32_bf16 v[148:151], v[116:119], v[52:55], v[148:151]\n"
      "ds_read_b128 v[116:119], v180 offset:8768\n"
      "s_waitcnt lgkmcnt(8)\n"
      "v_mfma_f32_16x16x32_bf16 v[144:147], v[120:123], v[24:27], v[144:147]\n"
      "v_mfma_f32_16x16x32_bf16 v[148:151], v[120:123], v[56:59], v[148:151]\n"
      "ds_read_b128 v[120:123], v180 offset:8832\n"
      "s_waitcnt lgkmcnt(8)\n"
      "v_mfma_f32_16x16x32_bf16 v[144:147], v[124:127], v[28:31], v[144:147]\n"
      "v_mfma_f32_16x16x32_bf16 v[148:151], v[124:127], v[60:63], v[148:151]\n"
      "ds_read_b128 v[124:127], v180 offset:8896\n"
      "s_waitcnt lgkmcnt(3)\n"
      "v_readfirstlane_b32 s40, v189\n"
      "v_add_u32 v180, s44, v180\n"
      "s_cmp_ge_i32 s40, s42\n"
      "s_cbranch_scc1 .Lp4e_%=\n"
      "s_mov_b32 s49, 0x400000\n"
      "s_branch .Lp4f_%=\n"
      ".Lp4_%=:\n"
      "s_sleep 1\n"
      ".Lp4f_%=:\n"
      "ds_read_b32 v189, v188 offset:64\n"
      "s_sub_u32 s49, s49, 1\n"
      "s_waitcnt lgkmcnt(0)\n"
      "v_readfirstlane_b32 s40, v189\n"
      "s_cbranch_scc1 .Lp4e_%=\n"
      "s_cmp_ge_i32 s40, s42\n"
      "s_cbranch_scc0 .Lp4_%=\n"
      ".Lp4e_%=:\n"
      "v_mfma_f32_16x16x32_bf16 v[152:155], v[96:99], v[0:3], 0\n"
      "ds_read_b64_tr_b16 v[128:129], v181 offset:0\n"
      "ds_read_b64_tr_b16 v[130:131], v181 offset:2048\n"
      "v_mfma_f32_16x16x32_bf16 v[156:159], v[96:99], v[32:35], 0\n"
      "v_fma_f32 v160, v144, s50, -v176\n"
      "v_add_u32 v181, s48, v181\n"
      "v_exp_f32 v160, v160\n"
      "v_mfma_f32_16x16x32_bf16 v[152:155], v[100:103], v[4:7], v[152:155]\n"
      "ds_read_b64_tr_b16 v[132:133], v182 offset:0\n"
      "ds_read_b64_tr_b16 v[134:135], v182 offset:2048\n"
      "v_mfma_f32_16x16x32_bf16 v[156:159], v[100:103], v[36:39], v[156:159]\n"
      "v_fma_f32 v161, v145, s50, -v176\n"
      "v_add_u32 v182, s48, v182\n"
      "v_exp_f32 v161, v161\n"
      "v_mfma_f32_16x16x32_bf16 v[152:155], v[104:107], v[8:11], v[152:155]\n"
      "ds_read_b64_tr_b16 v[136:137], v183 offset:0\n"
      "ds_read_b64_tr_b16 v[138:139], v183 offset:2048\n"
      "v_mfma_f32_16x16x32_bf16 v[156:159], v[104:107], v[40:43], v[156:159]\n"
      "v_fma_f32 v162, v146, s50, -v176\n"
      "v_add_u32 v183, s48, v183\n"
      "v_exp_f32 v162, v162\n"
      "v_mfma_f32_16x16x32_bf16 v[152:155], v[108:111], v[12:15], v[152:155]\n"
      "ds_read_b64_tr_b16 v[140:141], v184 offset:0\n"
      "ds_read_b64_tr_b16 v[142:143], v184 offset:2048\n"
      "v_mfma_f32_16x16x32_bf16 v[156:159], v[108:111], v[44:47], v[156:159]\n"
      "v_fma_f32 v163, v147, s50, -v176\n"
      "v_add_u32 v184, s48, v184\n"
      "v_exp_f32 v163, v163\n"
      "v_mfma_f32_16x16x32_bf16 v[152:155], v[112:115], v[16:19], v[152:155]\n"
      "ds_read_b64_tr_b16 v[224:225], v181 offset:0\n"
      "ds_read_b64_tr_b16 v[226:227], v181 offset:2048\n"
      "v_mfma_f32_16x16x32_bf16 v[156:159], v[112:115], v[48:51], v[156:159]\n"
      "v_fma_f32 v164, v148, s50, -v178\n"
      "v_add_u32 v181, s96, v181\n"
      "v_exp_f32 v164, v164\n"
      "s_waitcnt lgkmcnt(12)\n"
      "v_mfma_f32_16x16x32_bf16 v[152:155], v[116:119], v[20:23], v[152:155]\n"
      "ds_read_b64_tr_b16 v[228:229], v182 offset:0\n"
      "ds_read_b64_tr_b16 v[230:231], v182 offset:2048\n"
      "v_mfma_f32_16x16x32_bf16 v[156:159], v[116:119], v[52:55], v[156:159]\n"
      "v_fma_f32 v165, v149, s50, -v178\n"
      "v_add_u32 v182, s96, v182\n"
      "v_exp_f32 v165, v165\n"
      "s_waitcnt lgkmcnt(13)\n"
      "v_mfma_f32_16x16x32_bf16 v[152:155], v[120:123], v[24:27], v[152:155]\n"
      "ds_read_b64_tr_b16 v[232:233], v183 offset:0\n"
      "ds_read_b64_tr_b16 v[234:235], v183 offset:2048\n"
      "v_mfma_f32_16x16x32_bf16 v[156:159], v[120:123], v[56:59], v[156:159]\n"
      "v_fma_f32 v166, v150, s50, -v178\n"
      "v_add_u32 v183, s96, v183\n"
      "v_exp_f32 v166, v166\n"
      "s_waitcnt lgkmcnt(14)\n"
      "v_mfma_f32_16x16x32_bf16 v[152:155], v[124:127], v[28:31], v[152:155]\n"
      "ds_read_b64_tr_b16 v[236:237], v184 offset:0\n"
      "ds_read_b64_tr_b16 v[238:239], v184 offset:2048\n"
      "v_mfma_f32_16x16x32_bf16 v[156:159], v[124:127], v[60:63], v[156:159]\n"
      "v_fma_f32 v167, v151, s50, -v178\n"
      "v_add_u32 v184, s96, v184\n"
      "v_exp_f32 v167, v167\n"
      "ds_write_b32 v188, v186\n"
      "ds_read_b32 v189, v188 offset:64\n"
      "s_add_u32 s41, s41, s55\n"
      "s_add_u32 s42, s42, s55\n"
      "s_mov_b32 s55, 44\n"
      "v_cvt_pk_bf16_f32 v168, v160, v161\n"
      "v_cvt_pk_bf16_f32 v169, v162, v163\n"
      "v_cvt_pk_bf16_f32 v172, v164, v165\n"
      "v_cvt_pk_bf16_f32 v173, v166, v167\n"
      "v_add_u32 v186, 2, v186\n"
      "v_pk_fma_f32 v[160:161], v[152:153], s[50:51], v[176:177] op_sel_hi:[1,0,0] neg_lo:[0,0,1] neg_hi:[0,0,1]\n"
      "v_pk_fma_f32 v[162:163], v[154:155], s[50:51], v[176:177] op_sel_hi:[1,0,0] neg_lo:[0,0,1] neg_hi:[0,0,1]\n"
      "v_pk_fma_f32 v[164:165], v[156:157], s[50:51], v[178:179] op_sel_hi:[1,0,0] neg_lo:[0,0,1] neg_hi:[0,0,1]\n"
      "v_pk_fma_f32 v[166:167], v[158:159], s[50:51], v[178:179] op_sel_hi:[1,0,0] neg_lo:[0,0,1] neg_hi:[0,0,1]\n"
      "v_exp_f32 v160, v160\n"
      "v_exp_f32 v161, v161\n"
      "v_exp_f32 v162, v162\n"
      "v_exp_f32 v163, v163\n"
      "v_exp_f32 v164, v164\n"
      "v_exp_f32 v165, v165\n"
      "v_exp_f32 v166, v166\n"
      "v_exp_f32 v167, v167\n"
      "v_cvt_pk_bf16_f32 v170, v160, v161\n"
      "v_cvt_pk_bf16_f32 v171, v162, v163\n"
      "v_cvt_pk_bf16_f32 v174, v164, v165\n"
      "v_cvt_pk_bf16_f32 v175, v166, v167\n"
      ".LC1_%=:\n"
      "v_or3_b32 v160, v168, v169, v172\n"
      "v_or3_b32 v161, v173, v170, v171\n"
      "v_or3_b32 v160, v160, v161, v174\n"
      "v_bitop3_b32 v160, v160, s52, v175 bitop3:0xc8\n"
      "v_cmp_ne_u32 vcc, 0, v160\n"
      "s_waitcnt lgkmcnt(0)\n"
      "v_readfirstlane_b32 s40, v189\n"
      "s_cbranch_vccnz .LRARE_%=\n"
      ".LB1_%=:\n"
      "v_mfma_f32_16x16x32_bf16 v[80:83], v[128:131], v[172:175], v[80:83]\n"
      "v_dot2_f32_bf16 v179, v172, s53, v179\n"
      "v_mfma_f32_16x16x32_bf16 v[64:67], v[128:131], v[168:171], v[64:67]\n"
      "v_dot2_f32_bf16 v177, v168, s53, v177\n"
      "v_mfma_f32_16x16x32_bf16 v[84:87], v[132:135], v[172:175], v[84:87]\n"
      "v_dot2_f32_bf16 v179, v173, s53, v179\n"
      "v_mfma_f32_16x16x32_bf16 v[68:71], v[132:135], v[168:171], v[68:71]\n"
      "v_dot2_f32_bf16 v177, v169, s53, v177\n"
      "v_mfma_f32_16x16x32_bf16 v[88:91], v[136:139], v[172:175], v[88:91]\n"
      "v_dot2_f32_bf16 v179, v174, s53, v179\n"
      "v_mfma_f32_16x16x32_bf16 v[72:75], v[136:139], v[168:171], v[72:75]\n"
      "v_dot2_f32_bf16 v177, v170, s53, v177\n"
      "v_mfma_f32_16x16x32_bf16 v[92:95], v[140:143], v[172:175], v[92:95]\n"
      "v_dot2_f32_bf16 v179, v175, s53, v179\n"
      "v_mfma_f32_16x16x32_bf16 v[76:79], v[140:143], v[168:171], v[76:79]\n"
      "v_dot2_f32_bf16 v177, v171, s53, v177\n"
      "v_mfma_f32_16x16x32_bf16 v[208:211], v[224:227], v[172:175], v[208:211]\n"
      "v_mfma_f32_16x16x32_bf16 v[192:195], v[224:227], v[168:171], v[192:195]\n"
      "v_mfma_f32_16x16x32_bf16 v[212:215], v[228:231], v[172:175], v[212:215]\n"
      "v_mfma_f32_16x16x32_bf16 v[196:199], v[228:231], v[168:171], v[196:199]\n"
      "v_mfma_f32_16x16x32_bf16 v[216:219], v[232:235], v[172:175], v[216:219]\n"
      "v_mfma_f32_16x16x32_bf16 v[200:203], v[232:235], v[168:171], v[200:203]\n"
      "v_mfma_f32_16x16x32_bf16 v[220:223], v[236:239], v[172:175], v[220:223]\n"
      "v_mfma_f32_16x16x32_bf16 v[204:207], v[236:239], v[168:171], v[204:207]\n"
      "s_cmp_lg_u32 s41, s45\n"
      "s_cbranch_scc1 .LT1_%=\n"
      "s_cmp_eq_u32 s45, 766\n"
      "s_cbranch_scc1 .LBH_%=\n"
      "s_movk_i32 s45, 766\n"
      "s_waitcnt vmcnt(0)\n"
      "ds_read_b64 v[240:241], v244 offset:512\n"
      "ds_read_b64 v[242:243], v244\n"
      "ds_read_b128 v[96:99], v180 offset:20992\n"
      "ds_read_b128 v[100:103], v180 offset:21056\n"
      "ds_read_b128 v[104:107], v180 offset:21120\n"
      "ds_read_b128 v[108:111], v180 offset:21184\n"
      "ds_read_b128 v[112:115], v180 offset:21248\n"
      "ds_read_b128 v[116:119], v180 offset:21312\n"
      "ds_read_b128 v[120:123], v180 offset:21376\n"
      "ds_read_b128 v[124:127], v180 offset:21440\n"
      "s_waitcnt lgkmcnt(7)\n"
      "v_mfma_f32_16x16x32_bf16 v[144:147], v[96:99], v[0:3], 0\n"
      "s_mov_b32 s56, 0xff800000\n"
      "s_mov_b32 s57, 0xff800000\n"
      "v_mov_b64 v[152:153], s[56:57]\n"
      "v_lshlrev_b32 v160, 16, v240\n"
      "v_mfma_f32_16x16x32_bf16 v[148:151], v[96:99], v[32:35], 0\n"
      "ds_read_b64_tr_b16 v[128:129], v181 offset:12544\n"
      "v_sub_f32 v160, 1.0, v160\n"
      "s_waitcnt lgkmcnt(7)\n"
      "v_mfma_f32_16x16x32_bf16 v[144:147], v[100:103], v[4:7], v[144:147]\n"
      "v_mov_b64 v[154:155], s[56:57]\n"
      "v_and_b32 v161, 0xffff0000, v240\n"
      "v_mfma_f32_16x16x32_bf16 v[148:151], v[100:103], v[36:39], v[148:151]\n"
      "ds_read_b64_tr_b16 v[132:133], v182 offset:12544\n"
      "v_sub_f32 v161, 1.0, v161\n"
      "s_waitcnt lgkmcnt(7)\n"
      "v_mfma_f32_16x16x32_bf16 v[144:147], v[104:107], v[8:11], v[144:147]\n"
      "v_mov_b64 v[156:157], s[56:57]\n"
      "v_lshlrev_b32 v162, 16, v241\n"
      "v_mfma_f32_16x16x32_bf16 v[148:151], v[104:107], v[40:43], v[148:151]\n"
      "ds_read_b64_tr_b16 v[136:137], v183 offset:12544\n"
      "v_sub_f32 v162, 1.0, v162\n"
      "s_waitcnt lgkmcnt(7)\n"
      "v_mfma_f32_16x16x32_bf16 v[144:147], v[108:111], v[12:15], v[144:147]\n"
      "v_mov_b64 v[158:159], s[56:57]\n"
      "v_and_b32 v163, 0xffff0000, v241\n"
      "v_mfma_f32_16x16x32_bf16 v[148:151], v[108:111], v[44:47], v[148:151]\n"
      "ds_read_b64_tr_b16 v[140:141], v184 offset:12544\n"
      "v_sub_f32 v163, 1.0, v163\n"
      "s_waitcnt lgkmcnt(7)\n"
      "v_mfma_f32_16x16x32_bf16 v[144:147], v[112:115], v[16:19], v[144:147]\n"
      "v_lshlrev_b32 v164, 16, v242\n"
      "v_mfma_f32_16x16x32_bf16 v[148:151], v[112:115], v[48:51], v[148:151]\n"
      "ds_read_b64_tr_b16 v[224:225], v181 offset:14592\n"
      "v_sub_f32 v164, 1.0, v164\n"
      "s_waitcnt lgkmcnt(7)\n"
      "v_mfma_f32_16x16x32_bf16 v[144:147], v[116:119], v[20:23], v[144:147]\n"
      "v_and_b32 v165, 0xffff0000, v242\n"
      "v_mfma_f32_16x16x32_bf16 v[148:151], v[116:119], v[52:55], v[148:151]\n"
      "ds_read_b64_tr_b16 v[228:229], v182 offset:14592\n"
      "v_sub_f32 v165, 1.0, v165\n"
      "s_waitcnt lgkmcnt(7)\n"
      "v_mfma_f32_16x16x32_bf16 v[144:147], v[120:123], v[24:27], v[144:147]\n"
      "v_lshlrev_b32 v166, 16, v243\n"
      "v_mfma_f32_16x16x32_bf16 v[148:151], v[120:123], v[56:59], v[148:151]\n"
      "ds_read_b64_tr_b16 v[232:233], v183 offset:14592\n"
      "v_sub_f32 v166, 1.0, v166\n"
      "s_waitcnt lgkmcnt(7)\n"
      "v_mfma_f32_16x16x32_bf16 v[144:147], v[124:127], v[28:31], v[144:147]\n"
      "v_and_b32 v167, 0xffff0000, v243\n"
      "v_mfma_f32_16x16x32_bf16 v[148:151], v[124:127], v[60:63], v[148:151]\n"
      "ds_read_b64_tr_b16 v[236:237], v184 offset:14592\n"
      "v_sub_f32 v167, 1.0, v167\n"
      "ds_read_b32 v189, v188 offset:64\n"
      "v_mov_b64 v[170:171], 0\n"
      "v_mov_b64 v[174:175], 0\n"
      "s_nop 3\n"
      "v_fmac_f32 v144, 0xd06e6b28, v160\n"
      "v_fmac_f32 v145, 0xd06e6b28, v161\n"
      "v_fmac_f32 v146, 0xd06e6b28, v162\n"
      "v_fmac_f32 v147, 0xd06e6b28, v163\n"
      "v_fmac_f32 v148, 0xd06e6b28, v164\n"
      "v_fmac_f32 v149, 0xd06e6b28, v165\n"
      "v_fmac_f32 v150, 0xd06e6b28, v166\n"
      "v_fmac_f32 v151, 0xd06e6b28, v167\n"
      "v_pk_fma_f32 v[160:161], v[144:145], s[50:51], v[176:177] op_sel_hi:[1,0,0] neg_lo:[0,0,1] neg_hi:[0,0,1]\n"
      "v_pk_fma_f32 v[162:163], v[146:147], s[50:51], v[176:177] op_sel_hi:[1,0,0] neg_lo:[0,0,1] neg_hi:[0,0,1]\n"
      "v_pk_fma_f32 v[164:165], v[148:149], s[50:51], v[178:179] op_sel_hi:[1,0,0] neg_lo:[0,0,1] neg_hi:[0,0,1]\n"
      "v_pk_fma_f32 v[166:167], v[150:151], s[50:51], v[178:179] op_sel_hi:[1,0,0] neg_lo:[0,0,1] neg_hi:[0,0,1]\n"
      "v_exp_f32 v160, v160\n"
      "v_exp_f32 v161, v161\n"
      "v_exp_f32 v162, v162\n"
      "v_exp_f32 v163, v163\n"
      "v_exp_f32 v164, v164\n"
      "v_exp_f32 v165, v165\n"
      "v_exp_f32 v166, v166\n"
      "v_exp_f32 v167, v167\n"
      "s_nop 0\n"
      "v_cvt_pk_bf16_f32 v168, v160, v161\n"
      "v_cvt_pk_bf16_f32 v169, v162, v163\n"
      "v_cvt_pk_bf16_f32 v172, v164, v165\n"
      "v_cvt_pk_bf16_f32 v173, v166, v167\n"
      "s_branch .LC1_%=\n"
      ".LBH_%=:\n"
      "v_xor_b32 v160, 8, v188\n"
      "v_xor_b32 v161, 4, v188\n"
      "v_mov_b32 v162, 40\n"
      "s_mov_b32 s49, 0x400000\n"
      ".Lp5_%=:\n"
      "ds_read_b32 v189, v160\n"
      "s_sub_u32 s49, s49, 1\n"
      "s_waitcnt lgkmcnt(0)\n"
      "v_readfirstlane_b32 s54, v189\n"
      "s_cbranch_scc1 .Lp5e_%=\n"
      "s_cmp_ge_i32 s54, 31\n"
      "s_cbranch_scc0 .Lp5_%=\n"
      ".Lp5e_%=:\n"
      "s_nop 1\n"
      "ds_write_b128 v190, v[80:83] offset:0\n"
      "ds_write_b128 v190, v[84:87] offset:1024\n"
      "ds_write_b128 v190, v[88:91] offset:2048\n"
      "ds_write_b128 v190, v[92:95] offset:3072\n"
      "ds_write_b64 v190, v[178:179] offset:8192\n"
      "ds_write_b128 v190, v[208:211] offset:4096\n"
      "ds_write_b128 v190, v[212:215] offset:5120\n"
      "ds_write_b128 v190, v[216:219] offset:6144\n"
      "ds_write_b128 v190, v[220:223] offset:7168\n"
      "ds_write_b32 v188, v162\n"
      "v_mov_b64 v[0:1], v[192:193]\n"
      "v_mov_b64 v[2:3], v[194:195]\n"
      "v_mov_b64 v[4:5], v[196:197]\n"
      "v_mov_b64 v[6:7], v[198:199]\n"
      "v_mov_b64 v[8:9], v[200:201]\n"
      "v_mov_b64 v[10:11], v[202:203]\n"
      "v_mov_b64 v[12:13], v[204:205]\n"
      "v_mov_b64 v[14:15], v[206:207]\n"
      "v_subrev_u32 v181, 16896, v181\n"
      "v_subrev_u32 v182, 16896, v182\n"
      "v_subrev_u32 v183, 16896, v183\n"
      "v_subrev_u32 v184, 16896, v184\n"
      "s_mov_b32 s49, 0x400000\n"
      ".Lp6_%=:\n"
      "ds_read_b32 v189, v161\n"
      "s_sub_u32 s49, s49, 1\n"
      "s_waitcnt lgkmcnt(0)\n"
      "v_readfirstlane_b32 s54, v189\n"
      "s_cbranch_scc1 .Lp6e_%=\n"
      "s_cmp_ge_i32 s54, 40\n"
      "s_cbranch_scc0 .Lp6_%=\n"
      ".Lp6e_%=:\n"
      "ds_read_b32 v58, v188 offset:368\n"
      "ds_read_b64 v[56:57], v187 offset:4096\n"
      "ds_read_b128 v[96:99], v187 offset:0\n"
      "ds_read_b128 v[100:103], v187 offset:1024\n"
      "ds_read_b128 v[104:107], v187 offset:2048\n"
      "ds_read_b128 v[108:111], v187 offset:3072\n"
      "ds_read_b128 v[192:195], v185 offset:0\n"
      "ds_read_b128 v[196:199], v185 offset:1024\n"
      "ds_read_b128 v[200:203], v185 offset:2048\n"
      "ds_read_b128 v[204:207], v185 offset:3072\n"
      "ds_read_b128 v[208:211], v185 offset:4096\n"
      "ds_read_b128 v[212:215], v185 offset:5120\n"
      "ds_read_b128 v[216:219], v185 offset:6144\n"
      "ds_read_b128 v[220:223], v185 offset:7168\n"
      "ds_read_b128 v[224:227], v185 offset:8192\n"
      "s_waitcnt lgkmcnt(9)\n"
      "ds_read_b128 v[228:231], v185 offset:9216\n"
      "ds_read_b128 v[232:235], v185 offset:10240\n"
      "ds_read_b128 v[236:239], v185 offset:11264\n"
      "ds_read_b128 v[240:243], v185 offset:12288\n"
      "ds_read_b128 v[244:247], v185 offset:13312\n"
      "ds_read_b128 v[248:251], v185 offset:14336\n"
      "ds_read_b128 v[252:255], v185 offset:15360\n"
      "s_branch .LJOIN_%=\n"
      ".LT0_%=:\n"
      "s_sub_u32 s54, s41, 8\n"
      "s_cmp_ge_i32 s40, s54\n"
      "s_cbranch_scc1 .Lp7e_%=\n"
      "s_mov_b32 s49, 0x400000\n"
      "s_branch .Lp7f_%=\n"
      ".Lp7_%=:\n"
      "s_sleep 1\n"
      ".Lp7f_%=:\n"
      "ds_read_b32 v189, v188 offset:64\n"
      "s_sub_u32 s49, s49, 1\n"
      "s_waitcnt lgkmcnt(0)\n"
      "v_readfirstlane_b32 s40, v189\n"
      "s_cbranch_scc1 .Lp7e_%=\n"
      "s_cmp_ge_i32 s40, s54\n"
      "s_cbranch_scc0 .Lp7_%=\n"
      ".Lp7e_%=:\n"
      "ds_read_b128 v[96:99], v180 offset:0\n"
      "ds_read_b128 v[100:103], v180 offset:64\n"
      "ds_read_b128 v[104:107], v180 offset:128\n"
      "ds_read_b128 v[108:111], v180 offset:192\n"
      "ds_read_b128 v[112:115], v180 offset:256\n"
      "ds_read_b128 v[116:119], v180 offset:320\n"
      "ds_read_b128 v[120:123], v180 offset:384\n"
      "ds_read_b128 v[124:127], v180 offset:448\n"
      "s_cmp_ge_i32 s40, s41\n"
      "s_cbranch_scc1 .Lp8e_%=\n"
      "s_mov_b32 s49, 0x400000\n"
      "s_branch .Lp8f_%=\n"
      ".Lp8_%=:\n"
      "s_sleep 1\n"
      ".Lp8f_%=:\n"
      "ds_read_b32 v189, v188 offset:64\n"
      "s_sub_u32 s49, s49, 1\n"
      "s_waitcnt lgkmcnt(0)\n"
      "v_readfirstlane_b32 s40, v189\n"
      "s_cbranch_scc1 .Lp8e_%=\n"
      "s_cmp_ge_i32 s40, s41\n"
      "s_cbranch_scc0 .Lp8_%=\n"
      ".Lp8e_%=:\n"
      "s_waitcnt lgkmcnt(7)\n"
      "v_mfma_f32_16x16x32_bf16 v[144:147], v[96:99], v[0:3], 0\n"
      "s_add_u32 s54, s43, 41984\n"
      "s_cmp_ge_u32 s54, 104960\n"
      "s_movk_i32 s44, 0xa4\n"
      "v_mfma_f32_16x16x32_bf16 v[148:151], v[96:99], v[32:35], 0\n"
      "ds_read_b128 v[96:99], v180 offset:8448\n"
      "s_waitcnt lgkmcnt(7)\n"
      "v_mfma_f32_16x16x32_bf16 v[144:147], v[100:103], v[4:7], v[144:147]\n"
      "s_cmovk_i32 s44, 0xff0a\n"
      "s_lshl_b32 s44, s44, 8\n"
      "v_mfma_f32_16x16x32_bf16 v[148:151], v[100:103], v[36:39], v[148:151]\n"
      "ds_read_b128 v[100:103], v180 offset:8512\n"
      "s_waitcnt lgkmcnt(7)\n"
      "v_mfma_f32_16x16x32_bf16 v[144:147], v[104:107], v[8:11], v[144:147]\n"
      "s_add_u32 s43, s43, s44\n"
      "v_mfma_f32_16x16x32_bf16 v[148:151], v[104:107], v[40:43], v[148:151]\n"
      "ds_read_b128 v[104:107], v180 offset:8576\n"
      "s_waitcnt lgkmcnt(7)\n"
      "v_mfma_f32_16x16x32_bf16 v[144:147], v[108:111], v[12:15], v[144:147]\n"
      "v_mfma_f32_16x16x32_bf16 v[148:151], v[108:111], v[44:47], v[148:151]\n"
      "ds_read_b128 v[108:111], v180 offset:8640\n"
      "s_waitcnt lgkmcnt(7)\n"
      "v_mfma_f32_16x16x32_bf16 v[144:147], v[112:115], v[16:19], v[144:147]\n"
      "v_mfma_f32_16x16x32_bf16 v[148:151], v[112:115], v[48:51], v[148:151]\n"
      "ds_read_b128 v[112:115], v180 offset:8704\n"
      "ds_read_b32 v189, v188 offset:64\n"
      "s_waitcnt lgkmcnt(8)\n"
      "v_mfma_f32_16x16x32_bf16 v[144:147], v[116:119], v[20:23], v[144:147]\n"
      "v_mfma_f32_16x16x32_bf16 v[148:151], v[116:119], v[52:55], v[148:151]\n"
      "ds_read_b128 v[116:119], v180 offset:8768\n"
      "s_waitcnt lgkmcnt(8)\n"
      "v_mfma_f32_16x16x32_bf16 v[144:147], v[120:123], v[24:27], v[144:147]\n"
      "v_mfma_f32_16x16x32_bf16 v[148:151], v[120:123], v[56:59], v[148:151]\n"
      "ds_read_b128 v[120:123], v180 offset:8832\n"
      "s_waitcnt lgkmcnt(8)\n"
      "v_mfma_f32_16x16x32_bf16 v[144:147], v[124:127], v[28:31], v[144:147]\n"
      "v_mfma_f32_16x16x32_bf16 v[148:151], v[124:127], v[60:63], v[148:151]\n"
      "ds_read_b128 v[124:127], v180 offset:8896\n"
      "s_waitcnt lgkmcnt(3)\n"
      "v_readfirstlane_b32 s40, v189\n"
      "v_add_u32 v180, s44, v180\n"
      "s_cmp_ge_i32 s40, s42\n"
      "s_cbranch_scc1 .Lp9e_%=\n"
      "s_mov_b32 s49, 0x400000\n"
      "s_branch .Lp9f_%=\n"
      ".Lp9_%=:\n"
      "s_sleep 1\n"
      ".Lp9f_%=:\n"
      "ds_read_b32 v189, v188 offset:64\n"
      "s_sub_u32 s49, s49, 1\n"
      "s_waitcnt lgkmcnt(0)\n"
      "v_readfirstlane_b32 s40, v189\n"
      "s_cbranch_scc1 .Lp9e_%=\n"
      "s_cmp_ge_i32 s40, s42\n"
      "s_cbranch_scc0 .Lp9_%=\n"
      ".Lp9e_%=:\n"
      "v_mfma_f32_16x16x32_bf16 v[152:155], v[96:99], v[0:3], 0\n"
      "ds_read_b64_tr_b16 v[128:129], v181 offset:0\n"
      "v_fma_f32 v160, v144, s50, -v176\n"
      "v_mfma_f32_16x16x32_bf16 v[156:159], v[96:99], v[32:35], 0\n"
      "v_exp_f32 v160, v160\n"
      "v_mfma_f32_16x16x32_bf16 v[152:155], v[100:103], v[4:7], v[152:155]\n"
      "ds_read_b64_tr_b16 v[130:131], v181 offset:2048\n"
      "v_fma_f32 v161, v145, s50, -v176\n"
      "v_mfma_f32_16x16x32_bf16 v[156:159], v[100:103], v[36:39], v[156:159]\n"
      "v_exp_f32 v161, v161\n"
      "v_mfma_f32_16x16x32_bf16 v[152:155], v[104:107], v[8:11], v[152:155]\n"
      "ds_read_b64_tr_b16 v[132:133], v182 offset:0\n"
      "v_fma_f32 v162, v146, s50, -v176\n"
      "v_mfma_f32_16x16x32_bf16 v[156:159], v[104:107], v[40:43], v[156:159]\n"
      "v_exp_f32 v162, v162\n"
      "v_mfma_f32_16x16x32_bf16 v[152:155], v[108:111], v[12:15], v[152:155]\n"
      "ds_read_b64_tr_b16 v[134:135], v182 offset:2048\n"
      "v_fma_f32 v163, v147, s50, -v176\n"
      "v_mfma_f32_16x16x32_bf16 v[156:159], v[108:111], v[44:47], v[156:159]\n"
      "v_exp_f32 v163, v163\n"
      "v_mfma_f32_16x16x32_bf16 v[152:155], v[112:115], v[16:19], v[152:155]\n"
      "ds_read_b64_tr_b16 v[136:137], v183 offset:0\n"
      "v_fma_f32 v164, v148, s50, -v178\n"
      "v_mfma_f32_16x16x32_bf16 v[156:159], v[112:115], v[48:51], v[156:159]\n"
      "v_exp_f32 v164, v164\n"
      "s_waitcnt lgkmcnt(7)\n"
      "v_mfma_f32_16x16x32_bf16 v[152:155], v[116:119], v[20:23], v[152:155]\n"
      "ds_read_b64_tr_b16 v[138:139], v183 offset:2048\n"
      "v_fma_f32 v165, v149, s50, -v178\n"
      "v_mfma_f32_16x16x32_bf16 v[156:159], v[116:119], v[52:55], v[156:159]\n"
      "v_exp_f32 v165, v165\n"
      "s_waitcnt lgkmcnt(7)\n"
      "v_mfma_f32_16x16x32_bf16 v[152:155], v[120:123], v[24:27], v[152:155]\n"
      "ds_read_b64_tr_b16 v[140:141], v184 offset:0\n"
      "v_fma_f32 v166, v150, s50, -v178\n"
      "v_mfma_f32_16x16x32_bf16 v[156:159], v[120:123], v[56:59], v[156:159]\n"
      "v_exp_f32 v166, v166\n"
      "s_waitcnt lgkmcnt(7)\n"
      "v_mfma_f32_16x16x32_bf16 v[152:155], v[124:127], v[28:31], v[152:155]\n"
      "ds_read_b64_tr_b16 v[142:143], v184 offset:2048\n"
      "v_fma_f32 v167, v151, s50, -v178\n"
      "v_mfma_f32_16x16x32_bf16 v[156:159], v[124:127], v[60:63], v[156:159]\n"
      "v_exp_f32 v167, v167\n"
      "ds_write_b32 v188, v186\n"
      "ds_read_b32 v189, v188 offset:64\n"
      "s_add_u32 s41, s41, s55\n"
      "s_add_u32 s42, s42, s55\n"
      "s_mov_b32 s55, 44\n"
      "v_cvt_pk_bf16_f32 v168, v160, v161\n"
      "v_cvt_pk_bf16_f32 v169, v162, v163\n"
      "v_cvt_pk_bf16_f32 v172, v164, v165\n"
      "v_cvt_pk_bf16_f32 v173, v166, v167\n"
      "v_add_u32 v186, 2, v186\n"
      "v_pk_fma_f32 v[160:161], v[152:153], s[50:51], v[176:177] op_sel_hi:[1,0,0] neg_lo:[0,0,1] neg_hi:[0,0,1]\n"
      "v_pk_fma_f32 v[162:163], v[154:155], s[50:51], v[176:177] op_sel_hi:[1,0,0] neg_lo:[0,0,1] neg_hi:[0,0,1]\n"
      "v_pk_fma_f32 v[164:165], v[156:157], s[50:51], v[178:179] op_sel_hi:[1,0,0] neg_lo:[0,0,1] neg_hi:[0,0,1]\n"
      "v_pk_fma_f32 v[166:167], v[158:159], s[50:51], v[178:179] op_sel_hi:[1,0,0] neg_lo:[0,0,1] neg_hi:[0,0,1]\n"
      "v_exp_f32 v160, v160\n"
      "v_exp_f32 v161, v161\n"
      "v_exp_f32 v162, v162\n"
      "v_exp_f32 v163, v163\n"
      "v_exp_f32 v164, v164\n"
      "v_exp_f32 v165, v165\n"
      "v_exp_f32 v166, v166\n"
      "v_exp_f32 v167, v167\n"
      "v_add_u32 v181, s44, v181\n"
      "v_add_u32 v182, s44, v182\n"
      "v_add_u32 v183, s44, v183\n"
      "v_add_u32 v184, s44, v184\n"
      "v_cvt_pk_bf16_f32 v170, v160, v161\n"
      "v_cvt_pk_bf16_f32 v171, v162, v163\n"
      "v_cvt_pk_bf16_f32 v174, v164, v165\n"
      "v_cvt_pk_bf16_f32 v175, v166, v167\n"
      ".LC0_%=:\n"
      "v_or3_b32 v160, v168, v169, v172\n"
      "v_or3_b32 v161, v173, v170, v171\n"
      "v_or3_b32 v160, v160, v161, v174\n"
      "v_bitop3_b32 v160, v160, s52, v175 bitop3:0xc8\n"
      "v_cmp_ne_u32 vcc, 0, v160\n"
      "s_waitcnt lgkmcnt(0)\n"
      "v_readfirstlane_b32 s40, v189\n"
      "s_cbranch_vccnz .LRARE_%=\n"
      ".LB0_%=:\n"
      "v_mfma_f32_16x16x32_bf16 v[80:83], v[128:131], v[172:175], v[80:83]\n"
      "ds_write_b128 v185, v[172:175]\n"
      "v_dot2_f32_bf16 v179, v172, s53, v179\n"
      "v_mfma_f32_16x16x32_bf16 v[64:67], v[128:131], v[168:171], v[64:67]\n"
      "v_dot2_f32_bf16 v177, v168, s53, v177\n"
      "v_mfma_f32_16x16x32_bf16 v[84:87], v[132:135], v[172:175], v[84:87]\n"
      "v_dot2_f32_bf16 v179, v173, s53, v179\n"
      "s_set_gpr_idx_on s46, gpr_idx(DST)\n"
      "s_nop 0\n"
      "v_mov_b32 v192, v168\n"
      "v_mov_b32 v193, v169\n"
      "v_mov_b32 v194, v170\n"
      "v_mov_b32 v195, v171\n"
      "s_set_gpr_idx_off\n"
      "s_add_u32 s46, s46, 4\n"
      "v_mfma_f32_16x16x32_bf16 v[68:71], v[132:135], v[168:171], v[68:71]\n"
      "v_dot2_f32_bf16 v177, v169, s53, v177\n"
      "v_mfma_f32_16x16x32_bf16 v[88:91], v[136:139], v[172:175], v[88:91]\n"
      "v_dot2_f32_bf16 v179, v174, s53, v179\n"
      "v_add_u32 v185, 0x400, v185\n"
      "v_mfma_f32_16x16x32_bf16 v[72:75], v[136:139], v[168:171], v[72:75]\n"
      "v_dot2_f32_bf16 v177, v170, s53, v177\n"
      "v_mfma_f32_16x16x32_bf16 v[92:95], v[140:143], v[172:175], v[92:95]\n"
      "v_dot2_f32_bf16 v179, v175, s53, v179\n"
      "v_mfma_f32_16x16x32_bf16 v[76:79], v[140:143], v[168:171], v[76:79]\n"
      "v_dot2_f32_bf16 v177, v171, s53, v177\n"
      "s_cmp_lg_u32 s41, s45\n"
      "s_cbranch_scc1 .LT0_%=\n"
      "v_xor_b32 v161, 4, v188\n"
      "v_mov_b32 v162, 40\n"
      "ds_write_b64 v187, v[178:179] offset:4096\n"
      "ds_write_b128 v187, v[80:83] offset:0\n"
      "ds_write_b128 v187, v[84:87] offset:1024\n"
      "ds_write_b128 v187, v[88:91] offset:2048\n"
      "ds_write_b128 v187, v[92:95] offset:3072\n"
      "ds_write_b32 v188, v162\n"
      "ds_read_b32 v58, v188 offset:368\n"
      "ds_read_b32 v189, v161\n"
      "ds_read_b64 v[56:57], v190 offset:8192\n"
      "ds_read_b128 v[96:99], v190 offset:0\n"
      "ds_read_b128 v[100:103], v190 offset:1024\n"
      "ds_read_b128 v[104:107], v190 offset:2048\n"
      "ds_read_b128 v[108:111], v190 offset:3072\n"
      "ds_read_b128 v[0:3], v190 offset:4096\n"
      "ds_read_b128 v[4:7], v190 offset:5120\n"
      "ds_read_b128 v[8:11], v190 offset:6144\n"
      "ds_read_b128 v[12:15], v190 offset:7168\n"
      "v_subrev_u32 v181, 37888, v181\n"
      "v_subrev_u32 v182, 37888, v182\n"
      "v_subrev_u32 v183, 37888, v183\n"
      "v_subrev_u32 v184, 37888, v184\n"
      "s_waitcnt lgkmcnt(4)\n"
      "v_readfirstlane_b32 s54, v189\n"
      "s_cmp_lt_i32 s54, 40\n"
      "s_cbranch_scc1 .LKW_%=\n"
      ".LJOIN_%=:\n"
      "v_readfirstlane_b32 s97, v58\n"
      "v_max_f32 v52, v176, v56\n"
      "v_sub_f32 v58, v176, v52\n"
      "v_sub_f32 v59, v56, v52\n"
      "v_exp_f32 v58, v58\n"
      "v_exp_f32 v59, v59\n"
      "v_subrev_u32 v62, %[hb], v190\n"
      "s_add_u32 s97, s97, 1\n"
      "s_mul_i32 s98, s97, 0x9e3779b1\n"
      "s_xor_b32 s54, s97, 0x5851f42d\n"
      "s_mul_i32 s54, s54, 0x2c1b3c6d\n"
      "v_mul_f32 v53, v58, v177\n"
      "v_fma_f32 v53, v59, v57, v53\n"
      "v_mov_b32 v163, v53\n"
      "v_pk_mul_f32 v[96:97], v[96:97], v[58:59] op_sel:[0,1] op_sel_hi:[1,1]\n"
      "v_pk_mul_f32 v[98:99], v[98:99], v[58:59] op_sel:[0,1] op_sel_hi:[1,1]\n"
      "v_permlane16_swap_b32 v53, v163\n"
      "v_pk_fma_f32 v[96:97], v[64:65], v[58:59], v[96:97] op_sel_hi:[1,0,1]\n"
      "v_pk_fma_f32 v[98:99], v[66:67], v[58:59], v[98:99] op_sel_hi:[1,0,1]\n"
      "v_add_f32 v53, v53, v163\n"
      "v_mov_b32 v163, v53\n"
      "v_cvt_pk_bf16_f32 v44, v96, v97\n"
      "v_cvt_pk_bf16_f32 v45, v98, v99\n"
      "v_xor_b32 v44, s98, v44\n"
      "v_xor_b32 v45, s98, v45\n"
      "v_pk_mul_f32 v[100:101], v[100:101], v[58:59] op_sel:[0,1] op_sel_hi:[1,1]\n"
      "v_pk_mul_f32 v[102:103], v[102:103], v[58:59] op_sel:[0,1] op_sel_hi:[1,1]\n"
      "v_permlane32_swap_b32 v53, v163\n"
      "v_pk_fma_f32 v[100:101], v[68:69], v[58:59], v[100:101] op_sel_hi:[1,0,1]\n"
      "v_pk_fma_f32 v[102:103], v[70:71], v[58:59], v[102:103] op_sel_hi:[1,0,1]\n"
      "v_add_f32 v53, v53, v163\n"
      "v_cvt_pk_bf16_f32 v46, v100, v101\n"
      "v_cvt_pk_bf16_f32 v47, v102, v103\n"
      "v_xor_b32 v46, s98, v46\n"
      "v_xor_b32 v47, s98, v47\n"
      "buffer_store_dwordx4 v[44:47], v62, %[rm], %[to] offen offset:0 sc0 sc1\n"
      "v_pk_mul_f32 v[104:105], v[104:105], v[58:59] op_sel:[0,1] op_sel_hi:[1,1]\n"
      "v_pk_mul_f32 v[106:107], v[106:107], v[58:59] op_sel:[0,1] op_sel_hi:[1,1]\n"
      "v_pk_fma_f32 v[104:105], v[72:73], v[58:59], v[104:105] op_sel_hi:[1,0,1]\n"
      "v_pk_fma_f32 v[106:107], v[74:75], v[58:59], v[106:107] op_sel_hi:[1,0,1]\n"
      "v_cvt_pk_bf16_f32 v48, v104, v105\n"
      "v_cvt_pk_bf16_f32 v49, v106, v107\n"
      "v_xor_b32 v48, s98, v48\n"
      "v_xor_b32 v49, s98, v49\n"
      "v_pk_mul_f32 v[108:109], v[108:109], v[58:59] op_sel:[0,1] op_sel_hi:[1,1]\n"
      "v_pk_mul_f32 v[110:111], v[110:111], v[58:59] op_sel:[0,1] op_sel_hi:[1,1]\n"
      "v_pk_fma_f32 v[108:109], v[76:77], v[58:59], v[108:109] op_sel_hi:[1,0,1]\n"
      "v_pk_fma_f32 v[110:111], v[78:79], v[58:59], v[110:111] op_sel_hi:[1,0,1]\n"
      "v_cvt_pk_bf16_f32 v50, v108, v109\n"
      "v_cvt_pk_bf16_f32 v51, v110, v111\n"
      "v_xor_b32 v50, s98, v50\n"
      "v_xor_b32 v51, s98, v51\n"
      "buffer_store_dwordx4 v[48:51], v62, %[rm], %[to] offen offset:1024 sc0 sc1\n"
      "v_mov_b32 v186, s54\n"
      "v_dot2_u32_u16 v162, v52, s66, v186\n"
      "v_dot2_u32_u16 v160, v44, s58, 0\n"
      "v_dot2_u32_u16 v161, v48, s62, 0\n"
      "v_dot2_u32_u16 v160, v45, s59, v160\n"
      "v_dot2_u32_u16 v161, v49, s63, v161\n"
      "v_dot2_u32_u16 v160, v46, s60, v160\n"
      "v_dot2_u32_u16 v161, v50, s64, v161\n"
      "v_dot2_u32_u16 v160, v47, s61, v160\n"
      "v_dot2_u32_u16 v161, v51, s65, v161\n"
      "v_dot2_u32_u16 v162, v53, s67, v162\n"
      "ds_read_b32 v189, v188 offset:64\n"
      "v_and_b32 v63, 0xf0, v62\n"
      "v_add_u32 v160, v160, v161\n"
      "v_mov_b32 v161, v160\n"
      "s_nop 1\n"
      "v_permlane16_swap_b32 v160, v161\n"
      "v_add_u32 v160, v160, v161\n"
      "v_mov_b32 v161, v160\n"
      "s_nop 1\n"
      "v_permlane32_swap_b32 v160, v161\n"
      "v_add3_u32 v54, v160, v161, v162\n"
      "buffer_store_dwordx3 v[52:54], v63, %[rm], %[to] offen offset:2048 sc0 sc1\n"
      "s_waitcnt lgkmcnt(0)\n"
      "v_readfirstlane_b32 s40, v189\n"
      "s_cmp_ge_i32 s40, 754\n"
      "s_cbranch_scc1 .Lp10e_%=\n"
      "s_mov_b32 s49, 0x400000\n"
      ".Lp10_%=:\n"
      "ds_read_b32 v189, v188 offset:64\n"
      "s_sub_u32 s49, s49, 1\n"
      "s_waitcnt lgkmcnt(0)\n"
      "v_readfirstlane_b32 s40, v189\n"
      "s_cbranch_scc1 .Lp10e_%=\n"
      "s_cmp_ge_i32 s40, 754\n"
      "s_cbranch_scc0 .Lp10_%=\n"
      ".Lp10e_%=:\n"
      "ds_read_b64_tr_b16 v[128:129], v181 offset:0\n"
      "ds_read_b64_tr_b16 v[130:131], v181 offset:2048\n"
      "ds_read_b64_tr_b16 v[132:133], v182 offset:0\n"
      "ds_read_b64_tr_b16 v[134:135], v182 offset:2048\n"
      "ds_read_b64_tr_b16 v[136:137], v183 offset:0\n"
      "ds_read_b64_tr_b16 v[138:139], v183 offset:2048\n"
      "ds_read_b64_tr_b16 v[140:141], v184 offset:0\n"
      "ds_read_b64_tr_b16 v[142:143], v184 offset:2048\n"
      "v_cndmask_b32 v60, v59, v58, %[bm]\n"
      "v_cndmask_b32 v61, v58, v59, %[bm]\n"
      "v_lshlrev_b32 v180, 5, v63\n"
      "v_lshrrev_b32 v160, 3, v62\n"
      "v_and_or_b32 v180, v160, 32, v180\n"
      "v_lshrrev_b32 v160, 5, v62\n"
      "v_and_or_b32 v180, v160, 16, v180\n"
      "s_cmp_ge_i32 s40, 758\n"
      "s_cbranch_scc1 .Lp11e_%=\n"
      "s_mov_b32 s49, 0x400000\n"
      ".Lp11_%=:\n"
      "ds_read_b32 v189, v188 offset:64\n"
      "s_sub_u32 s49, s49, 1\n"
      "s_waitcnt lgkmcnt(0)\n"
      "v_readfirstlane_b32 s40, v189\n"
      "s_cbranch_scc1 .Lp11e_%=\n"
      "s_cmp_ge_i32 s40, 758\n"
      "s_cbranch_scc0 .Lp11_%=\n"
      ".Lp11e_%=:\n"
      "s_waitcnt lgkmcnt(6)\n"
      "v_mfma_f32_16x16x32_bf16 v[80:83], v[128:131], v[192:195], 0\n"
      "ds_read_b64_tr_b16 v[144:145], v181 offset:4096\n"
      "ds_read_b64_tr_b16 v[146:147], v181 offset:6144\n"
      "s_waitcnt lgkmcnt(6)\n"
      "v_mfma_f32_16x16x32_bf16 v[84:87], v[132:135], v[192:195], 0\n"
      "ds_read_b64_tr_b16 v[148:149], v182 offset:4096\n"
      "ds_read_b64_tr_b16 v[150:151], v182 offset:6144\n"
      "s_waitcnt lgkmcnt(6)\n"
      "v_mfma_f32_16x16x32_bf16 v[88:91], v[136:139], v[192:195], 0\n"
      "ds_read_b64_tr_b16 v[152:153], v183 offset:4096\n"
      "ds_read_b64_tr_b16 v[154:155], v183 offset:6144\n"
      "s_waitcnt lgkmcnt(6)\n"
      "v_mfma_f32_16x16x32_bf16 v[92:95], v[140:143], v[192:195], 0\n"
      "ds_read_b64_tr_b16 v[156:157], v184 offset:4096\n"
      "ds_read_b64_tr_b16 v[158:159], v184 offset:6144\n"
      "s_cmp_ge_i32 s40, 762\n"
      "s_cbranch_scc1 .Lp12e_%=\n"
      "s_mov_b32 s49, 0x400000\n"
      ".Lp12_%=:\n"
      "ds_read_b32 v189, v188 offset:64\n"
      "s_sub_u32 s49, s49, 1\n"
      "s_waitcnt lgkmcnt(0)\n"
      "v_readfirstlane_b32 s40, v189\n"
      "s_cbranch_scc1 .Lp12e_%=\n"
      "s_cmp_ge_i32 s40, 762\n"
      "s_cbranch_scc0 .Lp12_%=\n"
      ".Lp12e_%=:\n"
      "s_waitcnt lgkmcnt(6)\n"
      "v_mfma_f32_16x16x32_bf16 v[80:83], v[144:147], v[196:199], v[80:83]\n"
      "ds_read_b64_tr_b16 v[128:129], v181 offset:8192\n"
      "ds_read_b64_tr_b16 v[130:131], v181 offset:10240\n"
      "s_waitcnt lgkmcnt(6)\n"
      "v_mfma_f32_16x16x32_bf16 v[84:87], v[148:151], v[196:199], v[84:87]\n"
      "ds_read_b64_tr_b16 v[132:133], v182 offset:8192\n"
      "ds_read_b64_tr_b16 v[134:135], v182 offset:10240\n"
      "s_waitcnt lgkmcnt(6)\n"
      "v_mfma_f32_16x16x32_bf16 v[88:91], v[152:155], v[196:199], v[88:91]\n"
      "ds_read_b64_tr_b16 v[136:137], v183 offset:8192\n"
      "ds_read_b64_tr_b16 v[138:139], v183 offset:10240\n"
      "s_waitcnt lgkmcnt(6)\n"
      "v_mfma_f32_16x16x32_bf16 v[92:95], v[156:159], v[196:199], v[92:95]\n"
      "ds_read_b64_tr_b16 v[140:141], v184 offset:8192\n"
      "ds_read_b64_tr_b16 v[142:143], v184 offset:10240\n"
      "s_cmp_ge_i32 s40, 766\n"
      "s_cbranch_scc1 .Lp13e_%=\n"
      "s_mov_b32 s49, 0x400000\n"
      ".Lp13_%=:\n"
      "ds_read_b32 v189, v188 offset:64\n"
      "s_sub_u32 s49, s49, 1\n"
      "s_waitcnt lgkmcnt(0)\n"
      "v_readfirstlane_b32 s40, v189\n"
      "s_cbranch_scc1 .Lp13e_%=\n"
      "s_cmp_ge_i32 s40, 766\n"
      "s_cbranch_scc0 .Lp13_%=\n"
      ".Lp13e_%=:\n"
      "s_waitcnt lgkmcnt(6)\n"
      "v_mfma_f32_16x16x32_bf16 v[80:83], v[128:131], v[200:203], v[80:83]\n"
      "ds_read_b64_tr_b16 v[144:145], v181 offset:12288\n"
      "ds_read_b64_tr_b16 v[146:147], v181 offset:14336\n"
      "s_waitcnt lgkmcnt(6)\n"
      "v_mfma_f32_16x16x32_bf16 v[84:87], v[132:135], v[200:203], v[84:87]\n"
      "ds_read_b64_tr_b16 v[148:149], v182 offset:12288\n"
      "ds_read_b64_tr_b16 v[150:151], v182 offset:14336\n"
      "s_waitcnt lgkmcnt(6)\n"
      "v_mfma_f32_16x16x32_bf16 v[88:91], v[136:139], v[200:203], v[88:91]\n"
      "ds_read_b64_tr_b16 v[152:153], v183 offset:12288\n"
      "ds_read_b64_tr_b16 v[154:155], v183 offset:14336\n"
      "s_waitcnt lgkmcnt(6)\n"
      "v_mfma_f32_16x16x32_bf16 v[92:95], v[140:143], v[200:203], v[92:95]\n"
      "ds_read_b64_tr_b16 v[156:157], v184 offset:12288\n"
      "ds_read_b64_tr_b16 v[158:159], v184 offset:14336\n"
      "s_cmp_ge_i32 s40, 770\n"
      "s_cbranch_scc1 .Lp14e_%=\n"
      "s_mov_b32 s49, 0x400000\n"
      ".Lp14_%=:\n"
      "ds_read_b32 v189, v188 offset:64\n"
      "s_sub_u32 s49, s49, 1\n"
      "s_waitcnt lgkmcnt(0)\n"
      "v_readfirstlane_b32 s40, v189\n"
      "s_cbranch_scc1 .Lp14e_%=\n"
      "s_cmp_ge_i32 s40, 770\n"
      "s_cbranch_scc0 .Lp14_%=\n"
      ".Lp14e_%=:\n"
      "s_waitcnt lgkmcnt(6)\n"
      "v_mfma_f32_16x16x32_bf16 v[80:83], v[144:147], v[204:207], v[80:83]\n"
      "ds_read_b64_tr_b16 v[128:129], v181 offset:16384\n"
      "ds_read_b64_tr_b16 v[130:131], v181 offset:18432\n"
      "s_waitcnt lgkmcnt(6)\n"
      "v_mfma_f32_16x16x32_bf16 v[84:87], v[148:151], v[204:207], v[84:87]\n"
      "ds_read_b64_tr_b16 v[132:133], v182 offset:16384\n"
      "ds_read_b64_tr_b16 v[134:135], v182 offset:18432\n"
      "s_waitcnt lgkmcnt(6)\n"
      "v_mfma_f32_16x16x32_bf16 v[88:91], v[152:155], v[204:207], v[88:91]\n"
      "ds_read_b64_tr_b16 v[136:137], v183 offset:16384\n"
      "ds_read_b64_tr_b16 v[138:139], v183 offset:18432\n"
      "s_waitcnt lgkmcnt(6)\n"
      "v_mfma_f32_16x16x32_bf16 v[92:95], v[156:159], v[204:207], v[92:95]\n"
      "ds_read_b64_tr_b16 v[140:141], v184 offset:16384\n"
      "ds_read_b64_tr_b16 v[142:143], v184 offset:18432\n"
      "s_cmp_ge_i32 s40, 774\n"
      "s_cbranch_scc1 .Lp15e_%=\n"
      "s_mov_b32 s49, 0x400000\n"
      ".Lp15_%=:\n"
      "ds_read_b32 v189, v188 offset:64\n"
      "s_sub_u32 s49, s49, 1\n"
      "s_waitcnt lgkmcnt(0)\n"
      "v_readfirstlane_b32 s40, v189\n"
      "s_cbranch_scc1 .Lp15e_%=\n"
      "s_cmp_ge_i32 s40, 774\n"
      "s_cbranch_scc0 .Lp15_%=\n"
      ".Lp15e_%=:\n"
      "s_waitcnt lgkmcnt(6)\n"
      "v_mfma_f32_16x16x32_bf16 v[80:83], v[128:131], v[208:211], v[80:83]\n"
      "ds_read_b64_tr_b16 v[144:145], v181 offset:20480\n"
      "ds_read_b64_tr_b16 v[146:147], v181 offset:22528\n"
      "s_waitcnt lgkmcnt(6)\n"
      "v_mfma_f32_16x16x32_bf16 v[84:87], v[132:135], v[208:211], v[84:87]\n"
      "ds_read_b64_tr_b16 v[148:149], v182 offset:20480\n"
      "ds_read_b64_tr_b16 v[150:151], v182 offset:22528\n"
      "s_waitcnt lgkmcnt(6)\n"
      "v_mfma_f32_16x16x32_bf16 v[88:91], v[136:139], v[208:211], v[88:91]\n"
      "ds_read_b64_tr_b16 v[152:153], v183 offset:20480\n"
      "ds_read_b64_tr_b16 v[154:155], v183 offset:22528\n"
      "s_waitcnt lgkmcnt(6)\n"
      "v_mfma_f32_16x16x32_bf16 v[92:95], v[140:143], v[208:211], v[92:95]\n"
      "ds_read_b64_tr_b16 v[156:157], v184 offset:20480\n"
      "ds_read_b64_tr_b16 v[158:159], v184 offset:22528\n"
      "s_cmp_ge_i32 s40, 778\n"
      "s_cbranch_scc1 .Lp16e_%=\n"
      "s_mov_b32 s49, 0x400000\n"
      ".Lp16_%=:\n"
      "ds_read_b32 v189, v188 offset:64\n"
      "s_sub_u32 s49, s49, 1\n"
      "s_waitcnt lgkmcnt(0)\n"
      "v_readfirstlane_b32 s40, v189\n"
      "s_cbranch_scc1 .Lp16e_%=\n"
      "s_cmp_ge_i32 s40, 778\n"
      "s_cbranch_scc0 .Lp16_%=\n"
      ".Lp16e_%=:\n"
      "s_waitcnt lgkmcnt(6)\n"
      "v_mfma_f32_16x16x32_bf16 v[80:83], v[144:147], v[212:215], v[80:83]\n"
      "ds_read_b64_tr_b16 v[128:129], v181 offset:24576\n"
      "ds_read_b64_tr_b16 v[130:131], v181 offset:26624\n"
      "s_waitcnt lgkmcnt(6)\n"
      "v_mfma_f32_16x16x32_bf16 v[84:87], v[148:151], v[212:215], v[84:87]\n"
      "ds_read_b64_tr_b16 v[132:133], v182 offset:24576\n"
      "ds_read_b64_tr_b16 v[134:135], v182 offset:26624\n"
      "s_waitcnt lgkmcnt(6)\n"
      "v_mfma_f32_16x16x32_bf16 v[88:91], v[152:155], v[212:215], v[88:91]\n"
      "ds_read_b64_tr_b16 v[136:137], v183 offset:24576\n"
      "ds_read_b64_tr_b16 v[138:139], v183 offset:26624\n"
      "s_waitcnt lgkmcnt(6)\n"
      "v_mfma_f32_16x16x32_bf16 v[92:95], v[156:159], v[212:215], v[92:95]\n"
      "ds_read_b64_tr_b16 v[140:141], v184 offset:24576\n"
      "ds_read_b64_tr_b16 v[142:143], v184 offset:26624\n"
      "s_cmp_ge_i32 s40, 782\n"
      "s_cbranch_scc1 .Lp17e_%=\n"
      "s_mov_b32 s49, 0x400000\n"
      ".Lp17_%=:\n"
      "ds_read_b32 v189, v188 offset:64\n"
      "s_sub_u32 s49, s49, 1\n"
      "s_waitcnt lgkmcnt(0)\n"
      "v_readfirstlane_b32 s40, v189\n"
      "s_cbranch_scc1 .Lp17e_%=\n"
      "s_cmp_ge_i32 s40, 782\n"
      "s_cbranch_scc0 .Lp17_%=\n"
      ".Lp17e_%=:\n"
      "s_waitcnt lgkmcnt(6)\n"
      "v_mfma_f32_16x16x32_bf16 v[80:83], v[128:131], v[216:219], v[80:83]\n"
      "ds_read_b64_tr_b16 v[144:145], v181 offset:28672\n"
      "ds_read_b64_tr_b16 v[146:147], v181 offset:30720\n"
      "s_waitcnt lgkmcnt(6)\n"
      "v_mfma_f32_16x16x32_bf16 v[84:87], v[132:135], v[216:219], v[84:87]\n"
      "ds_read_b64_tr_b16 v[148:149], v182 offset:28672\n"
      "ds_read_b64_tr_b16 v[150:151], v182 offset:30720\n"
      "s_waitcnt lgkmcnt(6)\n"
      "v_mfma_f32_16x16x32_bf16 v[88:91], v[136:139], v[216:219], v[88:91]\n"
      "ds_read_b64_tr_b16 v[152:153], v183 offset:28672\n"
      "ds_read_b64_tr_b16 v[154:155], v183 offset:30720\n"
      "s_waitcnt lgkmcnt(6)\n"
      "v_mfma_f32_16x16x32_bf16 v[92:95], v[140:143], v[216:219], v[92:95]\n"
      "ds_read_b64_tr_b16 v[156:157], v184 offset:28672\n"
      "ds_read_b64_tr_b16 v[158:159], v184 offset:30720\n"
      "s_cmp_ge_i32 s40, 786\n"
      "s_cbranch_scc1 .Lp18e_%=\n"
      "s_mov_b32 s49, 0x400000\n"
      ".Lp18_%=:\n"
      "ds_read_b32 v189, v188 offset:64\n"
      "s_sub_u32 s49, s49, 1\n"
      "s_waitcnt lgkmcnt(0)\n"
      "v_readfirstlane_b32 s40, v189\n"
      "s_cbranch_scc1 .Lp18e_%=\n"
      "s_cmp_ge_i32 s40, 786\n"
      "s_cbranch_scc0 .Lp18_%=\n"
      ".Lp18e_%=:\n"
      "s_waitcnt lgkmcnt(6)\n"
      "v_mfma_f32_16x16x32_bf16 v[80:83], v[144:147], v[220:223], v[80:83]\n"
      "ds_read_b64_tr_b16 v[128:129], v181 offset:32768\n"
      "ds_read_b64_tr_b16 v[130:131], v181 offset:34816\n"
      "s_waitcnt lgkmcnt(6)\n"
      "v_mfma_f32_16x16x32_bf16 v[84:87], v[148:151], v[220:223], v[84:87]\n"
      "ds_read_b64_tr_b16 v[132:133], v182 offset:32768\n"
      "ds_read_b64_tr_b16 v[134:135], v182 offset:34816\n"
      "s_waitcnt lgkmcnt(6)\n"
      "v_mfma_f32_16x16x32_bf16 v[88:91], v[152:155], v[220:223], v[88:91]\n"
      "ds_read_b64_tr_b16 v[136:137], v183 offset:32768\n"
      "ds_read_b64_tr_b16 v[138:139], v183 offset:34816\n"
      "s_waitcnt lgkmcnt(6)\n"
      "v_mfma_f32_16x16x32_bf16 v[92:95], v[156:159], v[220:223], v[92:95]\n"
      "ds_read_b64_tr_b16 v[140:141], v184 offset:32768\n"
      "ds_read_b64_tr_b16 v[142:143], v184 offset:34816\n"
      "buffer_load_dwordx4 v[32:35], v62, %[rm], %[from] offen sc1\n"
      "buffer_load_dwordx4 v[36:39], v62, %[rm], %[from] offen offset:1024 sc1\n"
      "buffer_load_dwordx3 v[40:42], v63, %[rm], %[from] offen offset:2048 sc1\n"
      "s_cmp_ge_i32 s40, 790\n"
      "s_cbranch_scc1 .Lp19e_%=\n"
      "s_mov_b32 s49, 0x400000\n"
      ".Lp19_%=:\n"
      "ds_read_b32 v189, v188 offset:64\n"
      "s_sub_u32 s49, s49, 1\n"
      "s_waitcnt lgkmcnt(0)\n"
      "v_readfirstlane_b32 s40, v189\n"
      "s_cbranch_scc1 .Lp19e_%=\n"
      "s_cmp_ge_i32 s40, 790\n"
      "s_cbranch_scc0 .Lp19_%=\n"
      ".Lp19e_%=:\n"
      "s_waitcnt lgkmcnt(6)\n"
      "v_mfma_f32_16x16x32_bf16 v[80:83], v[128:131], v[224:227], v[80:83]\n"
      "ds_read_b64_tr_b16 v[144:145], v181 offset:36864\n"
      "ds_read_b64_tr_b16 v[146:147], v181 offset:38912\n"
      "s_waitcnt lgkmcnt(6)\n"
      "v_mfma_f32_16x16x32_bf16 v[84:87], v[132:135], v[224:227], v[84:87]\n"
      "ds_read_b64_tr_b16 v[148:149], v182 offset:36864\n"
      "ds_read_b64_tr_b16 v[150:151], v182 offset:38912\n"
      "s_waitcnt lgkmcnt(6)\n"
      "v_mfma_f32_16x16x32_bf16 v[88:91], v[136:139], v[224:227], v[88:91]\n"
      "ds_read_b64_tr_b16 v[152:153], v183 offset:36864\n"
      "ds_read_b64_tr_b16 v[154:155], v183 offset:38912\n"
      "s_waitcnt lgkmcnt(6)\n"
      "v_mfma_f32_16x16x32_bf16 v[92:95], v[140:143], v[224:227], v[92:95]\n"
      "ds_read_b64_tr_b16 v[156:157], v184 offset:36864\n"
      "ds_read_b64_tr_b16 v[158:159], v184 offset:38912\n"
      "s_cmp_ge_i32 s40, 794\n"
      "s_cbranch_scc1 .Lp20e_%=\n"
      "s_mov_b32 s49, 0x400000\n"
      ".Lp20_%=:\n"
      "ds_read_b32 v189, v188 offset:64\n"
      "s_sub_u32 s49, s49, 1\n"
      "s_waitcnt lgkmcnt(0)\n"
      "v_readfirstlane_b32 s40, v189\n"
      "s_cbranch_scc1 .Lp20e_%=\n"
      "s_cmp_ge_i32 s40, 794\n"
      "s_cbranch_scc0 .Lp20_%=\n"
      ".Lp20e_%=:\n"
      "s_waitcnt lgkmcnt(6)\n"
      "v_mfma_f32_16x16x32_bf16 v[80:83], v[144:147], v[228:231], v[80:83]\n"
      "ds_read_b64_tr_b16 v[128:129], v181 offset:40960\n"
      "ds_read_b64_tr_b16 v[130:131], v181 offset:43008\n"
      "s_waitcnt lgkmcnt(6)\n"
      "v_mfma_f32_16x16x32_bf16 v[84:87], v[148:151], v[228:231], v[84:87]\n"
      "ds_read_b64_tr_b16 v[132:133], v182 offset:40960\n"
      "ds_read_b64_tr_b16 v[134:135], v182 offset:43008\n"
      "s_waitcnt lgkmcnt(6)\n"
      "v_mfma_f32_16x16x32_bf16 v[88:91], v[152:155], v[228:231], v[88:91]\n"
      "ds_read_b64_tr_b16 v[136:137], v183 offset:40960\n"
      "ds_read_b64_tr_b16 v[138:139], v183 offset:43008\n"
      "s_waitcnt lgkmcnt(6)\n"
      "v_mfma_f32_16x16x32_bf16 v[92:95], v[156:159], v[228:231], v[92:95]\n"
      "ds_read_b64_tr_b16 v[140:141], v184 offset:40960\n"
      "ds_read_b64_tr_b16 v[142:143], v184 offset:43008\n"
      "s_cmp_ge_i32 s40, 798\n"
      "s_cbranch_scc1 .Lp21e_%=\n"
      "s_mov_b32 s49, 0x400000\n"
      ".Lp21_%=:\n"
      "ds_read_b32 v189, v188 offset:64\n"
      "s_sub_u32 s49, s49, 1\n"
      "s_waitcnt lgkmcnt(0)\n"
      "v_readfirstlane_b32 s40, v189\n"
      "s_cbranch_scc1 .Lp21e_%=\n"
      "s_cmp_ge_i32 s40, 798\n"
      "s_cbranch_scc0 .Lp21_%=\n"
      ".Lp21e_%=:\n"
      "s_waitcnt lgkmcnt(6)\n"
      "v_mfma_f32_16x16x32_bf16 v[80:83], v[128:131], v[232:235], v[80:83]\n"
      "ds_read_b64_tr_b16 v[144:145], v181 offset:45056\n"
      "ds_read_b64_tr_b16 v[146:147], v181 offset:47104\n"
      "s_waitcnt lgkmcnt(6)\n"
      "v_mfma_f32_16x16x32_bf16 v[84:87], v[132:135], v[232:235], v[84:87]\n"
      "ds_read_b64_tr_b16 v[148:149], v182 offset:45056\n"
      "ds_read_b64_tr_b16 v[150:151], v182 offset:47104\n"
      "s_waitcnt lgkmcnt(6)\n"
      "v_mfma_f32_16x16x32_bf16 v[88:91], v[136:139], v[232:235], v[88:91]\n"
      "ds_read_b64_tr_b16 v[152:153], v183 offset:45056\n"
      "ds_read_b64_tr_b16 v[154:155], v183 offset:47104\n"
      "s_waitcnt lgkmcnt(6)\n"
      "v_mfma_f32_16x16x32_bf16 v[92:95], v[140:143], v[232:235], v[92:95]\n"
      "ds_read_b64_tr_b16 v[156:157], v184 offset:45056\n"
      "ds_read_b64_tr_b16 v[158:159], v184 offset:47104\n"
      "s_cmp_ge_i32 s40, 802\n"
      "s_cbranch_scc1 .Lp22e_%=\n"
      "s_mov_b32 s49, 0x400000\n"
      ".Lp22_%=:\n"
      "ds_read_b32 v189, v188 offset:64\n"
      "s_sub_u32 s49, s49, 1\n"
      "s_waitcnt lgkmcnt(0)\n"
      "v_readfirstlane_b32 s40, v189\n"
      "s_cbranch_scc1 .Lp22e_%=\n"
      "s_cmp_ge_i32 s40, 802\n"
      "s_cbranch_scc0 .Lp22_%=\n"
      ".Lp22e_%=:\n"
      "s_waitcnt lgkmcnt(6)\n"
      "v_mfma_f32_16x16x32_bf16 v[80:83], v[144:147], v[236:239], v[80:83]\n"
      "ds_read_b64_tr_b16 v[128:129], v181 offset:49152\n"
      "ds_read_b64_tr_b16 v[130:131], v181 offset:51200\n"
      "s_waitcnt lgkmcnt(6)\n"
      "v_mfma_f32_16x16x32_bf16 v[84:87], v[148:151], v[236:239], v[84:87]\n"
      "ds_read_b64_tr_b16 v[132:133], v182 offset:49152\n"
      "ds_read_b64_tr_b16 v[134:135], v182 offset:51200\n"
      "s_waitcnt lgkmcnt(6)\n"
      "v_mfma_f32_16x16x32_bf16 v[88:91], v[152:155], v[236:239], v[88:91]\n"
      "ds_read_b64_tr_b16 v[136:137], v183 offset:49152\n"
      "ds_read_b64_tr_b16 v[138:139], v183 offset:51200\n"
      "s_waitcnt lgkmcnt(6)\n"
      "v_mfma_f32_16x16x32_bf16 v[92:95], v[156:159], v[236:239], v[92:95]\n"
      "ds_read_b64_tr_b16 v[140:141], v184 offset:49152\n"
      "ds_read_b64_tr_b16 v[142:143], v184 offset:51200\n"
      "s_cmp_ge_i32 s40, 806\n"
      "s_cbranch_scc1 .Lp23e_%=\n"
      "s_mov_b32 s49, 0x400000\n"
      ".Lp23_%=:\n"
      "ds_read_b32 v189, v188 offset:64\n"
      "s_sub_u32 s49, s49, 1\n"
      "s_waitcnt lgkmcnt(0)\n"
      "v_readfirstlane_b32 s40, v189\n"
      "s_cbranch_scc1 .Lp23e_%=\n"
      "s_cmp_ge_i32 s40, 806\n"
      "s_cbranch_scc0 .Lp23_%=\n"
      ".Lp23e_%=:\n"
      "s_waitcnt lgkmcnt(6)\n"
      "v_mfma_f32_16x16x32_bf16 v[80:83], v[128:131], v[240:243], v[80:83]\n"
      "ds_read_b64_tr_b16 v[144:145], v181 offset:53248\n"
      "ds_read_b64_tr_b16 v[146:147], v181 offset:55296\n"
      "s_waitcnt lgkmcnt(6)\n"
      "v_mfma_f32_16x16x32_bf16 v[84:87], v[132:135], v[240:243], v[84:87]\n"
      "ds_read_b64_tr_b16 v[148:149], v182 offset:53248\n"
      "ds_read_b64_tr_b16 v[150:151], v182 offset:55296\n"
      "s_waitcnt lgkmcnt(6)\n"
      "v_mfma_f32_16x16x32_bf16 v[88:91], v[136:139], v[240:243], v[88:91]\n"
      "ds_read_b64_tr_b16 v[152:153], v183 offset:53248\n"
      "ds_read_b64_tr_b16 v[154:155], v183 offset:55296\n"
      "s_waitcnt lgkmcnt(6)\n"
      "v_mfma_f32_16x16x32_bf16 v[92:95], v[140:143], v[240:243], v[92:95]\n"
      "ds_read_b64_tr_b16 v[156:157], v184 offset:53248\n"
      "ds_read_b64_tr_b16 v[158:159], v184 offset:55296\n"
      "s_cmp_ge_i32 s40, 810\n"
      "s_cbranch_scc1 .Lp24e_%=\n"
      "s_mov_b32 s49, 0x400000\n"
      ".Lp24_%=:\n"
      "ds_read_b32 v189, v188 offset:64\n"
      "s_sub_u32 s49, s49, 1\n"
      "s_waitcnt lgkmcnt(0)\n"
      "v_readfirstlane_b32 s40, v189\n"
      "s_cbranch_scc1 .Lp24e_%=\n"
      "s_cmp_ge_i32 s40, 810\n"
      "s_cbranch_scc0 .Lp24_%=\n"
      ".Lp24e_%=:\n"
      "s_waitcnt lgkmcnt(6)\n"
      "v_mfma_f32_16x16x32_bf16 v[80:83], v[144:147], v[244:247], v[80:83]\n"
      "ds_read_b64_tr_b16 v[128:129], v181 offset:57344\n"
      "ds_read_b64_tr_b16 v[130:131], v181 offset:59392\n"
      "s_waitcnt lgkmcnt(6)\n"
      "v_mfma_f32_16x16x32_bf16 v[84:87], v[148:151], v[244:247], v[84:87]\n"
      "ds_read_b64_tr_b16 v[132:133], v182 offset:57344\n"
      "ds_read_b64_tr_b16 v[134:135], v182 offset:59392\n"
      "s_waitcnt lgkmcnt(6)\n"
      "v_mfma_f32_16x16x32_bf16 v[88:91], v[152:155], v[244:247], v[88:91]\n"
      "ds_read_b64_tr_b16 v[136:137], v183 offset:57344\n"
      "ds_read_b64_tr_b16 v[138:139], v183 offset:59392\n"
      "s_waitcnt lgkmcnt(6)\n"
      "v_mfma_f32_16x16x32_bf16 v[92:95], v[156:159], v[244:247], v[92:95]\n"
      "ds_read_b64_tr_b16 v[140:141], v184 offset:57344\n"
      "ds_read_b64_tr_b16 v[142:143], v184 offset:59392\n"
      "s_waitcnt vmcnt(0)\n"
      "v_max_f32 v165, v52, v40\n"
      "v_sub_f32 v163, v52, v165\n"
      "v_sub_f32 v164, v40, v165\n"
      "v_exp_f32 v163, v163\n"
      "v_exp_f32 v164, v164\n"
      "v_dot2_u32_u16 v160, v32, s58, 0\n"
      "v_dot2_u32_u16 v161, v36, s62, 0\n"
      "v_dot2_u32_u16 v160, v33, s59, v160\n"
      "v_dot2_u32_u16 v161, v37, s63, v161\n"
      "v_dot2_u32_u16 v160, v34, s60, v160\n"
      "v_dot2_u32_u16 v161, v38, s64, v161\n"
      "v_dot2_u32_u16 v160, v35, s61, v160\n"
      "v_dot2_u32_u16 v161, v39, s65, v161\n"
      "v_dot2_u32_u16 v162, v40, s66, v186\n"
      "v_dot2_u32_u16 v162, v41, s67, v162\n"
      "v_mul_f32 v165, v163, v53\n"
      "v_fma_f32 v165, v164, v41, v165\n"
      "v_rcp_f32 v165, v165\n"
      "v_add_u32 v160, v160, v161\n"
      "v_mov_b32 v161, v160\n"
      "v_mul_f32 v163, v163, v165\n"
      "v_mul_f32 v164, v164, v165\n"
      "v_permlane16_swap_b32 v160, v161\n"
      "v_add_u32 v160, v160, v161\n"
      "v_mov_b32 v161, v160\n"
      "v_mul_f32 v165, v163, v60\n"
      "v_mul_f32 v166, v163, v61\n"
      "v_permlane32_swap_b32 v160, v161\n"
      "v_add3_u32 v160, v160, v161, v162\n"
      "v_cmp_ne_u32 vcc, v160, v42\n"
      "v_xor_b32_sdwa v16, v32, s98 dst_sel:WORD_1 dst_unused:UNUSED_PAD src0_sel:WORD_0 src1_sel:WORD_0\n"
      "v_xor_b32_sdwa v17, v32, s98 dst_sel:WORD_1 dst_unused:UNUSED_PAD src0_sel:WORD_1 src1_sel:WORD_1\n"
      "v_xor_b32_sdwa v18, v33, s98 dst_sel:WORD_1 dst_unused:UNUSED_PAD src0_sel:WORD_0 src1_sel:WORD_0\n"
      "v_xor_b32_sdwa v19, v33, s98 dst_sel:WORD_1 dst_unused:UNUSED_PAD src0_sel:WORD_1 src1_sel:WORD_1\n"
      "v_xor_b32_sdwa v20, v34, s98 dst_sel:WORD_1 dst_unused:UNUSED_PAD src0_sel:WORD_0 src1_sel:WORD_0\n"
      "v_xor_b32_sdwa v21, v34, s98 dst_sel:WORD_1 dst_unused:UNUSED_PAD src0_sel:WORD_1 src1_sel:WORD_1\n"
      "v_xor_b32_sdwa v22, v35, s98 dst_sel:WORD_1 dst_unused:UNUSED_PAD src0_sel:WORD_0 src1_sel:WORD_0\n"
      "v_xor_b32_sdwa v23, v35, s98 dst_sel:WORD_1 dst_unused:UNUSED_PAD src0_sel:WORD_1 src1_sel:WORD_1\n"
      "v_xor_b32_sdwa v24, v36, s98 dst_sel:WORD_1 dst_unused:UNUSED_PAD src0_sel:WORD_0 src1_sel:WORD_0\n"
      "v_xor_b32_sdwa v25, v36, s98 dst_sel:WORD_1 dst_unused:UNUSED_PAD src0_sel:WORD_1 src1_sel:WORD_1\n"
      "v_xor_b32_sdwa v26, v37, s98 dst_sel:WORD_1 dst_unused:UNUSED_PAD src0_sel:WORD_0 src1_sel:WORD_0\n"
      "v_xor_b32_sdwa v27, v37, s98 dst_sel:WORD_1 dst_unused:UNUSED_PAD src0_sel:WORD_1 src1_sel:WORD_1\n"
      "v_xor_b32_sdwa v28, v38, s98 dst_sel:WORD_1 dst_unused:UNUSED_PAD src0_sel:WORD_0 src1_sel:WORD_0\n"
      "v_xor_b32_sdwa v29, v38, s98 dst_sel:WORD_1 dst_unused:UNUSED_PAD src0_sel:WORD_1 src1_sel:WORD_1\n"
      "v_xor_b32_sdwa v30, v39, s98 dst_sel:WORD_1 dst_unused:UNUSED_PAD src0_sel:WORD_0 src1_sel:WORD_0\n"
      "v_xor_b32_sdwa v31, v39, s98 dst_sel:WORD_1 dst_unused:UNUSED_PAD src0_sel:WORD_1 src1_sel:WORD_1\n"
      "v_pk_mul_f32 v[16:17], v[16:17], v[164:165] op_sel_hi:[1,0]\n"
      "v_pk_mul_f32 v[18:19], v[18:19], v[164:165] op_sel_hi:[1,0]\n"
      "v_pk_mul_f32 v[20:21], v[20:21], v[164:165] op_sel_hi:[1,0]\n"
      "v_pk_mul_f32 v[22:23], v[22:23], v[164:165] op_sel_hi:[1,0]\n"
      "v_pk_mul_f32 v[24:25], v[24:25], v[164:165] op_sel_hi:[1,0]\n"
      "v_pk_mul_f32 v[26:27], v[26:27], v[164:165] op_sel_hi:[1,0]\n"
      "v_pk_mul_f32 v[28:29], v[28:29], v[164:165] op_sel_hi:[1,0]\n"
      "v_pk_mul_f32 v[30:31], v[30:31], v[164:165] op_sel_hi:[1,0]\n"
      "v_pk_fma_f32 v[16:17], v[0:1], v[164:165], v[16:17] op_sel:[0,1,0] op_sel_hi:[1,1,1]\n"
      "v_pk_fma_f32 v[18:19], v[2:3], v[164:165], v[18:19] op_sel:[0,1,0] op_sel_hi:[1,1,1]\n"
      "v_pk_fma_f32 v[20:21], v[4:5], v[164:165], v[20:21] op_sel:[0,1,0] op_sel_hi:[1,1,1]\n"
      "v_pk_fma_f32 v[22:23], v[6:7], v[164:165], v[22:23] op_sel:[0,1,0] op_sel_hi:[1,1,1]\n"
      "v_pk_fma_f32 v[24:25], v[8:9], v[164:165], v[24:25] op_sel:[0,1,0] op_sel_hi:[1,1,1]\n"
      "v_pk_fma_f32 v[26:27], v[10:11], v[164:165], v[26:27] op_sel:[0,1,0] op_sel_hi:[1,1,1]\n"
      "v_pk_fma_f32 v[28:29], v[12:13], v[164:165], v[28:29] op_sel:[0,1,0] op_sel_hi:[1,1,1]\n"
      "v_pk_fma_f32 v[30:31], v[14:15], v[164:165], v[30:31] op_sel:[0,1,0] op_sel_hi:[1,1,1]\n"
      "s_mov_b64 s[56:57], vcc\n"
      "s_cbranch_vccnz .LF2_%=\n"
      ".LF2b_%=:\n"
      "s_cmp_lg_u64 s[56:57], 0\n"
      "s_cbranch_scc1 .LRETRY_%=\n"
      ".LRB_%=:\n"
      "s_cmp_ge_i32 s40, 814\n"
      "s_cbranch_scc1 .Lp25e_%=\n"
      "s_mov_b32 s49, 0x400000\n"
      ".Lp25_%=:\n"
      "ds_read_b32 v189, v188 offset:64\n"
      "s_sub_u32 s49, s49, 1\n"
      "s_waitcnt lgkmcnt(0)\n"
      "v_readfirstlane_b32 s40, v189\n"
      "s_cbranch_scc1 .Lp25e_%=\n"
      "s_cmp_ge_i32 s40, 814\n"
      "s_cbranch_scc0 .Lp25_%=\n"
      ".Lp25e_%=:\n"
      "s_waitcnt lgkmcnt(6)\n"
      "v_mfma_f32_16x16x32_bf16 v[80:83], v[128:131], v[248:251], v[80:83]\n"
      "ds_read_b64_tr_b16 v[144:145], v181 offset:61440\n"
      "ds_read_b64_tr_b16 v[146:147], v181 offset:63488\n"
      "s_waitcnt lgkmcnt(6)\n"
      "v_mfma_f32_16x16x32_bf16 v[84:87], v[132:135], v[248:251], v[84:87]\n"
      "ds_read_b64_tr_b16 v[148:149], v182 offset:61440\n"
      "ds_read_b64_tr_b16 v[150:151], v182 offset:63488\n"
      "s_waitcnt lgkmcnt(6)\n"
      "v_mfma_f32_16x16x32_bf16 v[88:91], v[136:139], v[248:251], v[88:91]\n"
      "ds_read_b64_tr_b16 v[152:153], v183 offset:61440\n"
      "ds_read_b64_tr_b16 v[154:155], v183 offset:63488\n"
      "s_waitcnt lgkmcnt(6)\n"
      "v_mfma_f32_16x16x32_bf16 v[92:95], v[140:143], v[248:251], v[92:95]\n"
      "ds_read_b64_tr_b16 v[156:157], v184 offset:61440\n"
      "ds_read_b64_tr_b16 v[158:159], v184 offset:63488\n"
      "s_waitcnt lgkmcnt(6)\n"
      "v_mfma_f32_16x16x32_bf16 v[80:83], v[144:147], v[252:255], v[80:83]\n"
      "s_waitcnt lgkmcnt(4)\n"
      "v_mfma_f32_16x16x32_bf16 v[84:87], v[148:151], v[252:255], v[84:87]\n"
      "s_waitcnt lgkmcnt(2)\n"
      "v_mfma_f32_16x16x32_bf16 v[88:91], v[152:155], v[252:255], v[88:91]\n"
      "s_waitcnt lgkmcnt(0)\n"
      "v_mfma_f32_16x16x32_bf16 v[92:95], v[156:159], v[252:255], v[92:95]\n"
      "s_nop 1\n"
      "v_pk_fma_f32 v[16:17], v[80:81], v[166:167], v[16:17] op_sel_hi:[1,0,1]\n"
      "v_pk_fma_f32 v[18:19], v[82:83], v[166:167], v[18:19] op_sel_hi:[1,0,1]\n"
      "v_pk_fma_f32 v[20:21], v[84:85], v[166:167], v[20:21] op_sel_hi:[1,0,1]\n"
      "v_pk_fma_f32 v[22:23], v[86:87], v[166:167], v[22:23] op_sel_hi:[1,0,1]\n"
      "v_cvt_pk_bf16_f32 v112, v16, v17\n"
      "v_cvt_pk_bf16_f32 v114, v20, v21\n"
      "v_cvt_pk_bf16_f32 v113, v18, v19\n"
      "v_cvt_pk_bf16_f32 v115, v22, v23\n"
      "s_nop 0\n"
      "v_permlane16_swap_b32 v112, v114\n"
      "v_permlane16_swap_b32 v113, v115\n"
      "buffer_store_dwordx4 v[112:115], v180, %[ro], 0 offen offset:0 sc0 sc1\n"
      "v_pk_fma_f32 v[24:25], v[88:89], v[166:167], v[24:25] op_sel_hi:[1,0,1]\n"
      "v_pk_fma_f32 v[26:27], v[90:91], v[166:167], v[26:27] op_sel_hi:[1,0,1]\n"
      "v_pk_fma_f32 v[28:29], v[92:93], v[166:167], v[28:29] op_sel_hi:[1,0,1]\n"
      "v_pk_fma_f32 v[30:31], v[94:95], v[166:167], v[30:31] op_sel_hi:[1,0,1]\n"
      "v_cvt_pk_bf16_f32 v116, v24, v25\n"
      "v_cvt_pk_bf16_f32 v118, v28, v29\n"
      "v_cvt_pk_bf16_f32 v117, v26, v27\n"
      "v_cvt_pk_bf16_f32 v119, v30, v31\n"
      "s_nop 0\n"
      "v_permlane16_swap_b32 v116, v118\n"
      "v_permlane16_swap_b32 v117, v119\n"
      "buffer_store_dwordx4 v[116:119], v180, %[ro], 0 offen offset:64 sc0 sc1\n"
      "s_cmp_lg_u32 %[to], 0\n"
      "s_cbranch_scc1 .LEND_%=\n"
      "v_mov_b32 v160, 0\n"
      "v_mov_b32 v161, s97\n"
      "s_mov_b64 exec, 1\n"
      "global_store_dword v160, v161, %[ep] sc0 sc1 nt\n"
      ".LEND_%=:\n"
      "s_endpgm\n"
      ".LKW_%=:\n"
      "s_mov_b32 s49, 0x400000\n"
      ".Lp26_%=:\n"
      "ds_read_b32 v189, v161\n"
      "s_sub_u32 s49, s49, 1\n"
      "s_waitcnt lgkmcnt(0)\n"
      "v_readfirstlane_b32 s54, v189\n"
      "s_cbranch_scc1 .Lp26e_%=\n"
      "s_cmp_ge_i32 s54, 40\n"
      "s_cbranch_scc0 .Lp26_%=\n"
      ".Lp26e_%=:\n"
      "ds_read_b64 v[56:57], v190 offset:8192\n"
      "ds_read_b128 v[96:99], v190 offset:0\n"
      "ds_read_b128 v[100:103], v190 offset:1024\n"
      "ds_read_b128 v[104:107], v190 offset:2048\n"
      "ds_read_b128 v[108:111], v190 offset:3072\n"
      "ds_read_b128 v[0:3], v190 offset:4096\n"
      "ds_read_b128 v[4:7], v190 offset:5120\n"
      "ds_read_b128 v[8:11], v190 offset:6144\n"
      "ds_read_b128 v[12:15], v190 offset:7168\n"
      "s_waitcnt lgkmcnt(4)\n"
      "s_branch .LJOIN_%=\n"
      ".LF2_%=:\n"
      "buffer_load_dwordx4 v[32:35], v62, %[rm], %[from] offen sc1\n"
      "buffer_load_dwordx4 v[36:39], v62, %[rm], %[from] offen offset:1024 sc1\n"
      "buffer_load_dwordx3 v[40:42], v63, %[rm], %[from] offen offset:2048 sc1\n"
      "s_branch .LF2b_%=\n"
      ".LRETRY_%=:\n"
      "s_mov_b32 s49, 0x400000\n"
      ".LRT_%=:\n"
      "s_waitcnt vmcnt(0)\n"
      "v_max_f32 v165, v52, v40\n"
      "v_sub_f32 v163, v52, v165\n"
      "v_sub_f32 v164, v40, v165\n"
      "v_exp_f32 v163, v163\n"
      "v_exp_f32 v164, v164\n"
      "v_dot2_u32_u16 v160, v32, s58, 0\n"
      "v_dot2_u32_u16 v161, v36, s62, 0\n"
      "v_dot2_u32_u16 v160, v33, s59, v160\n"
      "v_dot2_u32_u16 v161, v37, s63, v161\n"
      "v_dot2_u32_u16 v160, v34, s60, v160\n"
      "v_dot2_u32_u16 v161, v38, s64, v161\n"
      "v_dot2_u32_u16 v160, v35, s61, v160\n"
      "v_dot2_u32_u16 v161, v39, s65, v161\n"
      "v_dot2_u32_u16 v162, v40, s66, v186\n"
      "v_dot2_u32_u16 v162, v41, s67, v162\n"
      "v_mul_f32 v165, v163, v53\n"
      "v_fma_f32 v165, v164, v41, v165\n"
      "v_rcp_f32 v165, v165\n"
      "v_add_u32 v160, v160, v161\n"
      "v_mov_b32 v161, v160\n"
      "v_mul_f32 v163, v163, v165\n"
      "v_mul_f32 v164, v164, v165\n"
      "v_permlane16_swap_b32 v160, v161\n"
      "v_add_u32 v160, v160, v161\n"
      "v_mov_b32 v161, v160\n"
      "v_mul_f32 v165, v163, v60\n"
      "v_mul_f32 v166, v163, v61\n"
      "v_permlane32_swap_b32 v160, v161\n"
      "v_add3_u32 v160, v160, v161, v162\n"
      "v_cmp_ne_u32 vcc, v160, v42\n"
      "v_xor_b32_sdwa v16, v32, s98 dst_sel:WORD_1 dst_unused:UNUSED_PAD src0_sel:WORD_0 src1_sel:WORD_0\n"
      "v_xor_b32_sdwa v17, v32, s98 dst_sel:WORD_1 dst_unused:UNUSED_PAD src0_sel:WORD_1 src1_sel:WORD_1\n"
      "v_xor_b32_sdwa v18, v33, s98 dst_sel:WORD_1 dst_unused:UNUSED_PAD src0_sel:WORD_0 src1_sel:WORD_0\n"
      "v_xor_b32_sdwa v19, v33, s98 dst_sel:WORD_1 dst_unused:UNUSED_PAD src0_sel:WORD_1 src1_sel:WORD_1\n"
      "v_xor_b32_sdwa v20, v34, s98 dst_sel:WORD_1 dst_unused:UNUSED_PAD src0_sel:WORD_0 src1_sel:WORD_0\n"
      "v_xor_b32_sdwa v21, v34, s98 dst_sel:WORD_1 dst_unused:UNUSED_PAD src0_sel:WORD_1 src1_sel:WORD_1\n"
      "v_xor_b32_sdwa v22, v35, s98 dst_sel:WORD_1 dst_unused:UNUSED_PAD src0_sel:WORD_0 src1_sel:WORD_0\n"
      "v_xor_b32_sdwa v23, v35, s98 dst_sel:WORD_1 dst_unused:UNUSED_PAD src0_sel:WORD_1 src1_sel:WORD_1\n"
      "v_xor_b32_sdwa v24, v36, s98 dst_sel:WORD_1 dst_unused:UNUSED_PAD src0_sel:WORD_0 src1_sel:WORD_0\n"
      "v_xor_b32_sdwa v25, v36, s98 dst_sel:WORD_1 dst_unused:UNUSED_PAD src0_sel:WORD_1 src1_sel:WORD_1\n"
      "v_xor_b32_sdwa v26, v37, s98 dst_sel:WORD_1 dst_unused:UNUSED_PAD src0_sel:WORD_0 src1_sel:WORD_0\n"
      "v_xor_b32_sdwa v27, v37, s98 dst_sel:WORD_1 dst_unused:UNUSED_PAD src0_sel:WORD_1 src1_sel:WORD_1\n"
      "v_xor_b32_sdwa v28, v38, s98 dst_sel:WORD_1 dst_unused:UNUSED_PAD src0_sel:WORD_0 src1_sel:WORD_0\n"
      "v_xor_b32_sdwa v29, v38, s98 dst_sel:WORD_1 dst_unused:UNUSED_PAD src0_sel:WORD_1 src1_sel:WORD_1\n"
      "v_xor_b32_sdwa v30, v39, s98 dst_sel:WORD_1 dst_unused:UNUSED_PAD src0_sel:WORD_0 src1_sel:WORD_0\n"
      "v_xor_b32_sdwa v31, v39, s98 dst_sel:WORD_1 dst_unused:UNUSED_PAD src0_sel:WORD_1 src1_sel:WORD_1\n"
      "v_pk_mul_f32 v[16:17], v[16:17], v[164:165] op_sel_hi:[1,0]\n"
      "v_pk_mul_f32 v[18:19], v[18:19], v[164:165] op_sel_hi:[1,0]\n"
      "v_pk_mul_f32 v[20:21], v[20:21], v[164:165] op_sel_hi:[1,0]\n"
      "v_pk_mul_f32 v[22:23], v[22:23], v[164:165] op_sel_hi:[1,0]\n"
      "v_pk_mul_f32 v[24:25], v[24:25], v[164:165] op_sel_hi:[1,0]\n"
      "v_pk_mul_f32 v[26:27], v[26:27], v[164:165] op_sel_hi:[1,0]\n"
      "v_pk_mul_f32 v[28:29], v[28:29], v[164:165] op_sel_hi:[1,0]\n"
      "v_pk_mul_f32 v[30:31], v[30:31], v[164:165] op_sel_hi:[1,0]\n"
      "v_pk_fma_f32 v[16:17], v[0:1], v[164:165], v[16:17] op_sel:[0,1,0] op_sel_hi:[1,1,1]\n"
      "v_pk_fma_f32 v[18:19], v[2:3], v[164:165], v[18:19] op_sel:[0,1,0] op_sel_hi:[1,1,1]\n"
      "v_pk_fma_f32 v[20:21], v[4:5], v[164:165], v[20:21] op_sel:[0,1,0] op_sel_hi:[1,1,1]\n"
      "v_pk_fma_f32 v[22:23], v[6:7], v[164:165], v[22:23] op_sel:[0,1,0] op_sel_hi:[1,1,1]\n"
      "v_pk_fma_f32 v[24:25], v[8:9], v[164:165], v[24:25] op_sel:[0,1,0] op_sel_hi:[1,1,1]\n"
      "v_pk_fma_f32 v[26:27], v[10:11], v[164:165], v[26:27] op_sel:[0,1,0] op_sel_hi:[1,1,1]\n"
      "v_pk_fma_f32 v[28:29], v[12:13], v[164:165], v[28:29] op_sel:[0,1,0] op_sel_hi:[1,1,1]\n"
      "v_pk_fma_f32 v[30:31], v[14:15], v[164:165], v[30:31] op_sel:[0,1,0] op_sel_hi:[1,1,1]\n"
      "s_cbranch_vccz .LRB_%=\n"
      "s_sleep 1\n"
      "buffer_load_dwordx4 v[32:35], v62, %[rm], %[from] offen sc1\n"
      "buffer_load_dwordx4 v[36:39], v62, %[rm], %[from] offen offset:1024 sc1\n"
      "buffer_load_dwordx3 v[40:42], v63, %[rm], %[from] offen offset:2048 sc1\n"
      "s_sub_u32 s49, s49, 1\n"
      "s_cbranch_scc0 .LRT_%=\n"
      "s_branch .LRB_%=\n"
      ".LRARE_%=:\n"
      "v_max3_f32 v160, v144, v145, v146\n"
      "v_max3_f32 v161, v152, v153, v154\n"
      "v_max3_f32 v160, v160, v147, v155\n"
      "v_max_f32 v160, v160, v161\n"
      "v_mov_b32 v161, 0x41a00000\n"
      "v_fma_f32 v160, v160, s50, v161\n"
      "v_max_f32 v160, v160, v176\n"
      "v_mov_b32 v161, v160\n"
      "s_nop 1\n"
      "v_permlane16_swap_b32 v160, v161\n"
      "v_max_f32 v160, v160, v161\n"
      "v_mov_b32 v161, v160\n"
      "s_nop 1\n"
      "v_permlane32_swap_b32 v160, v161\n"
      "v_max_f32 v160, v160, v161\n"
      "v_ceil_f32 v160, v160\n"
      "v_sub_f32 v161, v160, v176\n"
      "v_exp_f32 v162, -v161\n"
      "v_min_f32 v161, 0x437f0000, v161\n"
      "v_cvt_u32_f32 v161, v161\n"
      "v_mul_u32_u24 v164, 0x800080, v161\n"
      "v_mov_b32 v176, v160\n"
      "v_mul_f32 v177, v177, v162\n"
      "v_pk_mul_f32 v[64:65], v[64:65], v[162:163] op_sel_hi:[1,0]\n"
      "v_pk_mul_f32 v[66:67], v[66:67], v[162:163] op_sel_hi:[1,0]\n"
      "v_pk_mul_f32 v[68:69], v[68:69], v[162:163] op_sel_hi:[1,0]\n"
      "v_pk_mul_f32 v[70:71], v[70:71], v[162:163] op_sel_hi:[1,0]\n"
      "v_pk_mul_f32 v[72:73], v[72:73], v[162:163] op_sel_hi:[1,0]\n"
      "v_pk_mul_f32 v[74:75], v[74:75], v[162:163] op_sel_hi:[1,0]\n"
      "v_pk_mul_f32 v[76:77], v[76:77], v[162:163] op_sel_hi:[1,0]\n"
      "v_pk_mul_f32 v[78:79], v[78:79], v[162:163] op_sel_hi:[1,0]\n"
      "v_max3_f32 v160, v148, v149, v150\n"
      "v_max3_f32 v161, v156, v157, v158\n"
      "v_max3_f32 v160, v160, v151, v159\n"
      "v_max_f32 v160, v160, v161\n"
      "v_mov_b32 v161, 0x41a00000\n"
      "v_fma_f32 v160, v160, s50, v161\n"
      "v_max_f32 v160, v160, v178\n"
      "v_mov_b32 v161, v160\n"
      "s_nop 1\n"
      "v_permlane16_swap_b32 v160, v161\n"
      "v_max_f32 v160, v160, v161\n"
      "v_mov_b32 v161, v160\n"
      "s_nop 1\n"
      "v_permlane32_swap_b32 v160, v161\n"
      "v_max_f32 v160, v160, v161\n"
      "v_ceil_f32 v160, v160\n"
      "v_sub_f32 v161, v160, v178\n"
      "v_exp_f32 v163, -v161\n"
      "v_min_f32 v161, 0x437f0000, v161\n"
      "v_cvt_u32_f32 v161, v161\n"
      "v_mul_u32_u24 v165, 0x800080, v161\n"
      "v_mov_b32 v178, v160\n"
      "v_mul_f32 v179, v179, v163\n"
      "v_pk_mul_f32 v[80:81], v[80:81], v[162:163] op_sel:[0,1] op_sel_hi:[1,1]\n"
      "v_pk_mul_f32 v[82:83], v[82:83], v[162:163] op_sel:[0,1] op_sel_hi:[1,1]\n"
      "v_pk_mul_f32 v[84:85], v[84:85], v[162:163] op_sel:[0,1] op_sel_hi:[1,1]\n"
      "v_pk_mul_f32 v[86:87], v[86:87], v[162:163] op_sel:[0,1] op_sel_hi:[1,1]\n"
      "v_pk_mul_f32 v[88:89], v[88:89], v[162:163] op_sel:[0,1] op_sel_hi:[1,1]\n"
      "v_pk_mul_f32 v[90:91], v[90:91], v[162:163] op_sel:[0,1] op_sel_hi:[1,1]\n"
      "v_pk_mul_f32 v[92:93], v[92:93], v[162:163] op_sel:[0,1] op_sel_hi:[1,1]\n"
      "v_pk_mul_f32 v[94:95], v[94:95], v[162:163] op_sel:[0,1] op_sel_hi:[1,1]\n"
      "s_cmp_eq_u32 s99, 0\n"
      "s_cbranch_scc1 .LRK_%=\n"
      "v_pk_mul_f32 v[192:193], v[192:193], v[162:163] op_sel_hi:[1,0]\n"
      "v_pk_mul_f32 v[194:195], v[194:195], v[162:163] op_sel_hi:[1,0]\n"
      "v_pk_mul_f32 v[196:197], v[196:197], v[162:163] op_sel_hi:[1,0]\n"
      "v_pk_mul_f32 v[198:199], v[198:199], v[162:163] op_sel_hi:[1,0]\n"
      "v_pk_mul_f32 v[200:201], v[200:201], v[162:163] op_sel_hi:[1,0]\n"
      "v_pk_mul_f32 v[202:203], v[202:203], v[162:163] op_sel_hi:[1,0]\n"
      "v_pk_mul_f32 v[204:205], v[204:205], v[162:163] op_sel_hi:[1,0]\n"
      "v_pk_mul_f32 v[206:207], v[206:207], v[162:163] op_sel_hi:[1,0]\n"
      "v_pk_mul_f32 v[208:209], v[208:209], v[162:163] op_sel:[0,1] op_sel_hi:[1,1]\n"
      "v_pk_mul_f32 v[210:211], v[210:211], v[162:163] op_sel:[0,1] op_sel_hi:[1,1]\n"
      "v_pk_mul_f32 v[212:213], v[212:213], v[162:163] op_sel:[0,1] op_sel_hi:[1,1]\n"
      "v_pk_mul_f32 v[214:215], v[214:215], v[162:163] op_sel:[0,1] op_sel_hi:[1,1]\n"
      "v_pk_mul_f32 v[216:217], v[216:217], v[162:163] op_sel:[0,1] op_sel_hi:[1,1]\n"
      "v_pk_mul_f32 v[218:219], v[218:219], v[162:163] op_sel:[0,1] op_sel_hi:[1,1]\n"
      "v_pk_mul_f32 v[220:221], v[220:221], v[162:163] op_sel:[0,1] op_sel_hi:[1,1]\n"
      "v_pk_mul_f32 v[222:223], v[222:223], v[162:163] op_sel:[0,1] op_sel_hi:[1,1]\n"
      ".LRE_%=:\n"
      "v_pk_fma_f32 v[160:161], v[144:145], s[50:51], v[176:177] op_sel_hi:[1,0,0] neg_lo:[0,0,1] neg_hi:[0,0,1]\n"
      "v_pk_fma_f32 v[162:163], v[146:147], s[50:51], v[176:177] op_sel_hi:[1,0,0] neg_lo:[0,0,1] neg_hi:[0,0,1]\n"
      "v_pk_fma_f32 v[164:165], v[148:149], s[50:51], v[178:179] op_sel_hi:[1,0,0] neg_lo:[0,0,1] neg_hi:[0,0,1]\n"
      "v_pk_fma_f32 v[166:167], v[150:151], s[50:51], v[178:179] op_sel_hi:[1,0,0] neg_lo:[0,0,1] neg_hi:[0,0,1]\n"
      "v_exp_f32 v160, v160\n"
      "v_exp_f32 v161, v161\n"
      "v_exp_f32 v162, v162\n"
      "v_exp_f32 v163, v163\n"
      "v_exp_f32 v164, v164\n"
      "v_exp_f32 v165, v165\n"
      "v_exp_f32 v166, v166\n"
      "v_exp_f32 v167, v167\n"
      "s_nop 0\n"
      "v_cvt_pk_bf16_f32 v168, v160, v161\n"
      "v_cvt_pk_bf16_f32 v169, v162, v163\n"
      "v_cvt_pk_bf16_f32 v172, v164, v165\n"
      "v_cvt_pk_bf16_f32 v173, v166, v167\n"
      "v_pk_fma_f32 v[160:161], v[152:153], s[50:51], v[176:177] op_sel_hi:[1,0,0] neg_lo:[0,0,1] neg_hi:[0,0,1]\n"
      "v_pk_fma_f32 v[162:163], v[154:155], s[50:51], v[176:177] op_sel_hi:[1,0,0] neg_lo:[0,0,1] neg_hi:[0,0,1]\n"
      "v_pk_fma_f32 v[164:165], v[156:157], s[50:51], v[178:179] op_sel_hi:[1,0,0] neg_lo:[0,0,1] neg_hi:[0,0,1]\n"
      "v_pk_fma_f32 v[166:167], v[158:159], s[50:51], v[178:179] op_sel_hi:[1,0,0] neg_lo:[0,0,1] neg_hi:[0,0,1]\n"
      "v_exp_f32 v160, v160\n"
      "v_exp_f32 v161, v161\n"
      "v_exp_f32 v162, v162\n"
      "v_exp_f32 v163, v163\n"
      "v_exp_f32 v164, v164\n"
      "v_exp_f32 v165, v165\n"
      "v_exp_f32 v166, v166\n"
      "v_exp_f32 v167, v167\n"
      "s_nop 0\n"
      "v_cvt_pk_bf16_f32 v170, v160, v161\n"
      "v_cvt_pk_bf16_f32 v171, v162, v163\n"
      "v_cvt_pk_bf16_f32 v174, v164, v165\n"
      "v_cvt_pk_bf16_f32 v175, v166, v167\n"
      "s_cmp_eq_u32 s99, 0\n"
      "s_cbranch_scc1 .LB0_%=\n"
      "s_branch .LB1_%=\n"
      ".LRK_%=:\n"
      "s_cmp_eq_u32 s46, 0\n"
      "s_cbranch_scc1 .LRE_%=\n"
      "v_pk_sub_u16 v192, v192, v164 clamp\n"
      "v_pk_sub_u16 v193, v193, v164 clamp\n"
      "v_pk_sub_u16 v194, v194, v164 clamp\n"
      "v_pk_sub_u16 v195, v195, v164 clamp\n"
      "v_pk_sub_u16 v196, v196, v164 clamp\n"
      "v_pk_sub_u16 v197, v197, v164 clamp\n"
      "v_pk_sub_u16 v198, v198, v164 clamp\n"
      "v_pk_sub_u16 v199, v199, v164 clamp\n"
      "v_pk_sub_u16 v200, v200, v164 clamp\n"
      "v_pk_sub_u16 v201, v201, v164 clamp\n"
      "v_pk_sub_u16 v202, v202, v164 clamp\n"
      "v_pk_sub_u16 v203, v203, v164 clamp\n"
      "v_pk_sub_u16 v204, v204, v164 clamp\n"
      "v_pk_sub_u16 v205, v205, v164 clamp\n"
      "v_pk_sub_u16 v206, v206, v164 clamp\n"
      "v_pk_sub_u16 v207, v207, v164 clamp\n"
      "v_pk_sub_u16 v208, v208, v164 clamp\n"
      "v_pk_sub_u16 v209, v209, v164 clamp\n"
      "v_pk_sub_u16 v210, v210, v164 clamp\n"
      "v_pk_sub_u16 v211, v211, v164 clamp\n"
      "v_pk_sub_u16 v212, v212, v164 clamp\n"
      "v_pk_sub_u16 v213, v213, v164 clamp\n"
      "v_pk_sub_u16 v214, v214, v164 clamp\n"
      "v_pk_sub_u16 v215, v215, v164 clamp\n"
      "v_pk_sub_u16 v216, v216, v164 clamp\n"
      "v_pk_sub_u16 v217, v217, v164 clamp\n"
      "v_pk_sub_u16 v218, v218, v164 clamp\n"
      "v_pk_sub_u16 v219, v219, v164 clamp\n"
      "v_pk_sub_u16 v220, v220, v164 clamp\n"
      "v_pk_sub_u16 v221, v221, v164 clamp\n"
      "v_pk_sub_u16 v222, v222, v164 clamp\n"
      "v_pk_sub_u16 v223, v223, v164 clamp\n"
      "v_pk_sub_u16 v224, v224, v164 clamp\n"
      "v_pk_sub_u16 v225, v225, v164 clamp\n"
      "v_pk_sub_u16 v226, v226, v164 clamp\n"
      "v_pk_sub_u16 v227, v227, v164 clamp\n"
      "v_pk_sub_u16 v228, v228, v164 clamp\n"
      "v_pk_sub_u16 v229, v229, v164 clamp\n"
      "v_pk_sub_u16 v230, v230, v164 clamp\n"
      "v_pk_sub_u16 v231, v231, v164 clamp\n"
      "v_pk_sub_u16 v232, v232, v164 clamp\n"
      "v_pk_sub_u16 v233, v233, v164 clamp\n"
      "v_pk_sub_u16 v234, v234, v164 clamp\n"
      "v_pk_sub_u16 v235, v235, v164 clamp\n"
      "v_pk_sub_u16 v236, v236, v164 clamp\n"
      "v_pk_sub_u16 v237, v237, v164 clamp\n"
      "v_pk_sub_u16 v238, v238, v164 clamp\n"
      "v_pk_sub_u16 v239, v239, v164 clamp\n"
      "v_pk_sub_u16 v240, v240, v164 clamp\n"
      "v_pk_sub_u16 v241, v241, v164 clamp\n"
      "v_pk_sub_u16 v242, v242, v164 clamp\n"
      "v_pk_sub_u16 v243, v243, v164 clamp\n"
      "v_pk_sub_u16 v244, v244, v164 clamp\n"
      "v_pk_sub_u16 v245, v245, v164 clamp\n"
      "v_pk_sub_u16 v246, v246, v164 clamp\n"
      "v_pk_sub_u16 v247, v247, v164 clamp\n"
      "v_pk_sub_u16 v248, v248, v164 clamp\n"
      "v_pk_sub_u16 v249, v249, v164 clamp\n"
      "v_pk_sub_u16 v250, v250, v164 clamp\n"
      "v_pk_sub_u16 v251, v251, v164 clamp\n"
      "s_mov_b32 s49, s46\n"
      "v_mov_b32 v160, v185\n"
      ".LRL_%=:\n"
      "v_subrev_u32 v160, 0x400, v160\n"
      "ds_read_b128 v[168:171], v160\n"
      "s_sub_u32 s49, s49, 4\n"
      "s_cmp_lg_u32 s49, 0\n"
      "s_waitcnt lgkmcnt(0)\n"
      "v_pk_sub_u16 v168, v168, v165 clamp\n"
      "v_pk_sub_u16 v169, v169, v165 clamp\n"
      "v_pk_sub_u16 v170, v170, v165 clamp\n"
      "v_pk_sub_u16 v171, v171, v165 clamp\n"
      "ds_write_b128 v160, v[168:171]\n"
      "s_nop 1\n"
      "s_cbranch_scc1 .LRL_%=\n"
      "s_branch .LRE_%=\n"
      ".Lta_end:\n"
      : "+{v[168:171]}"(xv), "+{v[176:191]}"(mv)
      : [par] "s"(par), [qn] "s"(16 + 16 * hd), [hb] "s"(hb), [bm] "s"(0ull - par), [rm] "s"(rmsg), [ro] "s"(rout),
        [to] "s"(ks * MSG_STRIDE + cw * 2304), [from] "s"((1 - ks) * MSG_STRIDE + cw * 2304), [ep] "s"(ws + (head * WS_HEAD + cs * 128))
      : "memory", "scc", "vcc", "v0", "v1", "v2", "v3", "v4", "v5", "v6", "v7", "v8", "v9", "v10", "v11", "v12", "v13", "v14", "v15", "v16", "v17", "v18", "v19", "v20", "v21", "v22", "v23", "v24", "v25", "v26", "v27", "v28", "v29", "v30", "v31", "v32", "v33", "v34", "v35", "v36", "v37", "v38", "v39", "v40", "v41", "v42", "v43", "v44", "v45", "v46", "v47", "v48", "v49", "v50", "v51", "v52", "v53", "v54", "v55", "v56", "v57", "v58", "v59", "v60", "v61", "v62", "v63", "v64", "v65", "v66", "v67", "v68", "v69", "v70", "v71", "v72", "v73", "v74", "v75", "v76", "v77", "v78", "v79", "v80", "v81", "v82", "v83", "v84", "v85", "v86", "v87", "v88", "v89", "v90", "v91", "v92", "v93", "v94", "v95", "v96", "v97", "v98", "v99", "v100", "v101", "v102", "v103", "v104", "v105", "v106", "v107", "v108", "v109", "v110", "v111", "v112", "v113", "v114", "v115", "v116", "v117", "v118", "v119", "v120", "v121", "v122", "v123", "v124", "v125", "v126", "v127", "v128", "v129", "v130", "v131", "v132", "v133", "v134", "v135", "v136", "v137", "v138", "v139", "v140", "v141", "v142", "v143", "v144", "v145", "v146", "v147", "v148", "v149", "v150", "v151", "v152", "v153", "v154", "v155", "v156", "v157", "v158", "v159", "v160", "v161", "v162", "v163", "v164", "v165", "v166", "v167", "v172", "v173", "v174", "v175", "v192", "v193", "v194", "v195", "v196", "v197", "v198", "v199", "v200", "v201", "v202", "v203", "v204", "v205", "v206", "v207", "v208", "v209", "v210", "v211", "v212", "v213", "v214", "v215", "v216", "v217", "v218", "v219", "v220", "v221", "v222", "v223", "v224", "v225", "v226", "v227", "v228", "v229", "v230", "v231", "v232", "v233", "v234", "v235", "v236", "v237", "v238", "v239", "v240", "v241", "v242", "v243", "v244", "v245", "v246", "v247", "v248", "v249", "v250", "v251", "v252", "v253", "v254", "v255", "s40", "s41", "s42", "s43", "s44", "s45", "s46", "s47", "s48", "s49", "s50", "s51", "s52", "s53", "s54", "s55", "s56", "s57", "s58", "s59", "s60", "s61", "s62", "s63", "s64", "s65", "s66", "s67", "s68", "s69", "s70", "s71", "s72", "s73", "s74", "s75", "s76", "s77", "s78", "s79", "s80", "s81", "s82", "s83", "s84", "s85", "s86", "s87", "s88", "s89", "s90", "s91", "s92", "s93", "s94", "s95", "s96", "s97", "s98", "s99");
}

extern "C" void kernel_launch(void* const* d_in, const int* in_sizes, int n_in, void* d_out, int out_size, void* d_ws, size_t ws_size,
                              hipStream_t stream) {
  (void)in_sizes; (void)n_in; (void)out_size; (void)ws_size;
  (void)hipFuncSetAttribute((const void*)tree_attn_kernel, hipFuncAttributeMaxDynamicSharedMemorySize, LDS_BYTES);
  tree_attn_kernel<<<256, 320, LDS_BYTES, stream>>>((const char*)d_in[1], (const char*)d_in[2], (const char*)d_in[3], (const char*)d_in[4],
                                                   (const char*)d_in[0], (const char*)d_in[5], (char*)d_ws, (char*)d_out);
}
```
